```python
import torch

def _softmax(x, axis=-1):
    return torch.softmax(x, dim=axis)
def _gelu(x):
    return torch.nn.functional.gelu(x, approximate="tanh")
def _silu(x):
    return torch.nn.functional.silu(x)
def _sigmoid(x):
    return torch.sigmoid(x)
def _layernorm(x, axis=-1, eps=1e-5):
    m = x.mean(dim=axis, keepdim=True)
    v = x.var(dim=axis, keepdim=True, unbiased=False)
    return (x - m) / torch.sqrt(v + eps)
def _scatter_add(tgt, idx, src, axis=0):
    return tgt.clone().index_add_(axis, idx, src)
def _segment_sum(src, seg, n):
    out = torch.zeros((n,) + src.shape[1:], dtype=src.dtype)
    return out.index_add_(0, seg, src)
def _pad(x, pw):
    flat = tuple(v for pair in reversed(pw) for v in pair)
    return torch.nn.functional.pad(x, flat)

kernel_name = 'standalone__mha_block_cached__near_ridge'

def setup_inputs():

    _w = (lambda a: (lambda t: t.to(torch.bfloat16) if t.is_floating_point() else t)(torch.as_tensor(a)))
    B, Sq, D = 8, 128, 1024
    H, Sk, Dh = 32, 2048, 128
    return {
        "x": _w((lambda *s: torch.randn(*s, dtype=torch.bfloat16) * 0.8)(B, Sq, D)),
        "k_cache": _w((lambda *s: torch.randn(*s, dtype=torch.bfloat16) * 0.8)(B, H, Sk, Dh)),
        "v_cache": _w((lambda *s: torch.randn(*s, dtype=torch.bfloat16) * 0.05)(B, H, Sk, Dh)),
        "wq": _w((lambda *s: torch.randn(*s, dtype=torch.bfloat16) * 0.8)(D, H * Dh) * (D ** -0.5)),
        "wo": _w((lambda *s: torch.randn(*s, dtype=torch.bfloat16) * 0.05)(H * Dh, D) * ((H * Dh) ** -0.5)),
    }

def reference(x, k_cache, v_cache, wq, wo):
    B, Sq, D = x.shape
    H, Dh = 32, 128
    q = (lambda x, *d: x.permute(*d))((x @ wq).reshape(B, Sq, H, Dh), 0, 2, 1, 3)
    scores = torch.einsum("bhqd,bhkd->bhqk", q, k_cache) * (Dh ** -0.5)
    p = _softmax(scores, axis=-1)
    o = torch.einsum("bhqk,bhkd->bhqd", p, v_cache)
    return (lambda x, *d: x.permute(*d))(o, 0, 2, 1, 3).reshape(B, Sq, H * Dh) @ wo
```

```cpp
#include <hip/hip_runtime.h>
#include <hip/hip_bf16.h>
#include <stdint.h>

typedef float f32x16 __attribute__((ext_vector_type(16)));
typedef float f32x4 __attribute__((ext_vector_type(4)));
typedef float f32x2 __attribute__((ext_vector_type(2)));
typedef __bf16 bf16x8 __attribute__((ext_vector_type(8)));
typedef __bf16 bf16x2 __attribute__((ext_vector_type(2)));
typedef short s16x4 __attribute__((ext_vector_type(4)));
typedef short s16x8 __attribute__((ext_vector_type(8)));
typedef unsigned u32x4 __attribute__((ext_vector_type(4)));
typedef unsigned u32x2 __attribute__((ext_vector_type(2)));
typedef __amdgpu_buffer_rsrc_t rsrc_t;

#define LAS __attribute__((address_space(3)))
#define DI __device__ __forceinline__
#define MFMA(a, b, c) __builtin_amdgcn_mfma_f32_32x32x16_bf16((a), (b), (c), 0, 0, 0)
#define SBAR() __builtin_amdgcn_sched_barrier(0)

constexpr int NB = 8, SQ = 128, DM = 1024, NH = 32, SK = 2048, DH = 128;
constexpr int NT = SK / 64;
constexpr unsigned SLOT = 32768;
constexpr int LDS1 = 5 * 32768;
constexpr int LDS2 = 5 * 32768;
constexpr float CEXP = 0.12751743082459868f;

DI unsigned rfl(unsigned x) { return __builtin_amdgcn_readfirstlane(x); }
DI rsrc_t mkrsrc(const void* p, unsigned bytes) {
  return __builtin_amdgcn_make_buffer_rsrc((void*)p, (short)0, (int)bytes, 0x00020000);
}
template <int NTF>
DI void dma(unsigned dst, unsigned voff, rsrc_t rs, unsigned soff) {
  if constexpr (NTF)
    asm volatile("s_mov_b32 m0, %0\n\ts_nop 0\n\tbuffer_load_dwordx4 %1, %2, %3 offen nt lds" ::"s"(dst), "v"(voff), "s"(rs), "s"(soff) : "memory");
  else
    asm volatile("s_mov_b32 m0, %0\n\ts_nop 0\n\tbuffer_load_dwordx4 %1, %2, %3 offen lds" ::"s"(dst), "v"(voff), "s"(rs), "s"(soff) : "memory");
}
template <int N>
DI void wait_bar() { asm volatile("s_waitcnt vmcnt(%0) lgkmcnt(0)\n\ts_barrier" ::"i"(N) : "memory"); }
template <int N>
DI void wait_vm() { asm volatile("s_waitcnt vmcnt(%0) lgkmcnt(0)" ::"i"(N) : "memory"); }

DI bf16x8 lds_b128(unsigned addr) { return *(const LAS bf16x8*)(uintptr_t)addr; }
DI s16x4 lds_tr(unsigned addr) { return __builtin_amdgcn_ds_read_tr16_b64_v4i16((LAS s16x4*)(uintptr_t)addr); }
DI bf16x8 cat(s16x4 a, s16x4 b) { return __builtin_bit_cast(bf16x8, __builtin_shufflevector(a, b, 0, 1, 2, 3, 4, 5, 6, 7)); }
DI unsigned cvtpk(float lo, float hi) {
  f32x2 v = {lo, hi};
  return __builtin_bit_cast(unsigned, __builtin_convertvector(v, bf16x2));
}
template <int S>
DI bf16x8 pack8(const f32x16& v) {
  u32x4 w = {cvtpk(v[8 * S + 0], v[8 * S + 1]), cvtpk(v[8 * S + 2], v[8 * S + 3]), cvtpk(v[8 * S + 4], v[8 * S + 5]), cvtpk(v[8 * S + 6], v[8 * S + 7])};
  return __builtin_bit_cast(bf16x8, w);
}
DI float swap_max(float v) {
  auto rr = __builtin_amdgcn_permlane32_swap(__float_as_uint(v), __float_as_uint(v), false, false);
  return fmaxf(__uint_as_float(rr[0]), __uint_as_float(rr[1]));
}
DI float swap_sum(float v) {
  auto rr = __builtin_amdgcn_permlane32_swap(__float_as_uint(v), __float_as_uint(v), false, false);
  return __uint_as_float(rr[0]) + __uint_as_float(rr[1]);
}

__global__ __launch_bounds__(512, 2) void attn_kernel(const uint16_t* __restrict__ x, const uint16_t* __restrict__ kc,
                                                      const uint16_t* __restrict__ vc, const uint16_t* __restrict__ wq,
                                                      uint16_t* __restrict__ ofrag) {
  extern __shared__ __attribute__((aligned(16))) char smem[];
  const unsigned tid = threadIdx.x, lane = tid & 63;
  const unsigned wid = rfl(tid >> 6), g = wid >> 2, r = wid & 3;
  const unsigned l32 = lane & 31, hi = lane >> 5;
  const unsigned bid = blockIdx.x, xcd = bid & 7, idx = bid >> 3;
  const unsigned h = 4 * xcd + (idx & 3), b = idx >> 2;
  const unsigned lds0 = rfl((unsigned)(uintptr_t)smem);

  const unsigned l4 = lane >> 4;
  const size_t kvoff = (size_t)(b * NH + h) * SK * DH;
  const rsrc_t rsKV = mkrsrc((g ? vc : kc) + kvoff, SK * DH * 2);
  const rsrc_t rsQ = g ? mkrsrc(wq + h * DH, DM * NH * DH * 2 - h * DH * 2) : mkrsrc(x + (size_t)b * SQ * DM, SQ * DM * 2);
  const unsigned voffKV = (4 * r + l4) * 256 + 16 * ((lane & 15) ^ (g ? (l4 << 2) : (4 * r + l4)));
  const unsigned voffQ = g ? (4 * r + l4) * 8192 + 16 * ((lane & 15) ^ (l4 << 2))
                           : (8 * r + (lane >> 3)) * 2048 + 16 * ((lane & 7) ^ (4 * (r & 1) + l4));
  const unsigned qstS = g ? 64 * 8192 : 128, qstJ = g ? 16 * 8192 : 32 * 2048;
  const unsigned dstw = lds0 + g * 16384 + r * 1024;
  const unsigned kstart = (2 * b + 4 * (h & 3)) & 15;

  auto issueQ = [&](unsigned i, unsigned slot) {
    const unsigned so = ((i + kstart) & 15) * qstS;
#pragma unroll
    for (int j = 0; j < 4; ++j) dma<0>(dstw + slot * SLOT + j * 4096, voffQ, rsQ, so + j * qstJ);
  };
  auto issueKV = [&](unsigned t, unsigned slot) {
#pragma unroll
    for (int j = 0; j < 4; ++j) dma<1>(dstw + slot * SLOT + j * 4096, voffKV, rsKV, t * 16384 + j * 4096);
  };

  asm volatile("s_nop 4" ::: "memory");
  issueQ(0, 3); issueQ(1, 2); issueQ(2, 4);
  issueKV(0, 0); issueKV(1, 1);

  const unsigned q4 = (lane & 15) >> 2, p4 = lane & 3, blk = (lane >> 4) & 1;
  const unsigned xrow = 32 * r + l32;
  const unsigned baseX = lds0 + 128 * xrow + 16 * (hi ^ ((xrow >> 1) & 7));
  const unsigned baseW = lds0 + 16384 + 256 * (8 * hi + q4) + 16 * (4 * ((2 * g) ^ q4) + 2 * blk + (p4 & 1)) + 8 * (p4 >> 1);

  f32x16 qa0 = {}, qa1 = {};
  auto qstage = [&](unsigned slot) {
    const unsigned sx = baseX + slot * SLOT, sw0 = baseW + slot * SLOT, sw1 = sw0 ^ 64;
#pragma unroll
    for (int s = 0; s < 4; ++s) {
      const bf16x8 xb = lds_b128(sx ^ (s << 5));
      const bf16x8 w0 = cat(lds_tr(sw0 + 4096 * s), lds_tr(sw0 + 4096 * s + 1024));
      const bf16x8 w1 = cat(lds_tr(sw1 + 4096 * s), lds_tr(sw1 + 4096 * s + 1024));
      qa0 = MFMA(w0, xb, qa0);
      qa1 = MFMA(w1, xb, qa1);
    }
  };
  wait_bar<16>(); qstage(3);
  wait_bar<12>(); issueQ(3, 3); qstage(2);
  wait_bar<12>(); issueQ(4, 2); qstage(4);
  {
    unsigned scur = 3, snext = 2, sprev = 4;
    for (unsigned i = 3; i < 14; ++i) {
      wait_bar<4>();
      issueQ(i + 2, sprev);
      qstage(scur);
      const unsigned tmp = sprev; sprev = scur; scur = snext; snext = tmp;
    }
  }
  wait_bar<4>(); issueKV(2, 2); qstage(4);
  wait_bar<4>(); qstage(3);
  bf16x8 qf[8];
  {
    const unsigned xw = lds0 + 4 * SLOT + (r * 8 + 4 * g) * 1024 + lane * 16;
    *(LAS bf16x8*)(uintptr_t)(xw) = pack8<0>(qa0);
    *(LAS bf16x8*)(uintptr_t)(xw + 1024) = pack8<1>(qa0);
    *(LAS bf16x8*)(uintptr_t)(xw + 2048) = pack8<0>(qa1);
    *(LAS bf16x8*)(uintptr_t)(xw + 3072) = pack8<1>(qa1);
    wait_bar<63>();
    issueKV(3, 3);
    const unsigned xr = lds0 + 4 * SLOT + r * 8192 + lane * 16;
#pragma unroll
    for (int f = 0; f < 8; ++f) qf[f] = lds_b128(xr + f * 1024);
  }

  const unsigned baseK = lds0 + 8192 * g + 256 * l32 + 16 * (hi ^ (l32 & 15));
  const unsigned baseV = lds0 + 16384 + 8192 * g + 256 * (4 * hi + q4) + 16 * (4 * q4 + 2 * blk + (p4 >> 1)) + 8 * (p4 & 1);
  f32x16 o0 = {}, o1 = {}, o2 = {}, o3 = {};
  float m = -__builtin_inff(), l = 0.f;

  auto step = [&](unsigned slot) {
    const unsigned sk = baseK + slot * SLOT, sv = baseV + slot * SLOT;
    f32x16 s = {};
#pragma unroll
    for (int k = 0; k < 8; ++k) s = MFMA(lds_b128(sk ^ (k << 5)), qf[k], s);
    bf16x8 vf[4][2];
#pragma unroll
    for (int db = 0; db < 4; ++db)
#pragma unroll
      for (int ks = 0; ks < 2; ++ks) {
        const unsigned a = (sv ^ (db << 6)) + 4096 * ks;
        vf[db][ks] = cat(lds_tr(a), lds_tr(a + 2048));
      }
    float pm = fmaxf(fmaxf(s[0], s[1]), s[2]);
#pragma unroll
    for (int i = 3; i < 15; i += 2) pm = fmaxf(fmaxf(pm, s[i]), s[i + 1]);
    pm = fmaxf(pm, s[15]);
    pm = swap_max(pm);
    if (__any(pm > m)) {
      const float mn = fmaxf(m, pm);
      const float alpha = __builtin_amdgcn_exp2f((m - mn) * CEXP);
      m = mn;
      l *= alpha;
      o0 *= alpha; o1 *= alpha; o2 *= alpha; o3 *= alpha;
    }
    const float nm = -m * CEXP;
    float sum = 0.f;
#pragma unroll
    for (int i = 0; i < 16; ++i) { s[i] = __builtin_amdgcn_exp2f(__builtin_fmaf(s[i], CEXP, nm)); sum += s[i]; }
    l += sum;
    const bf16x8 p0 = pack8<0>(s), p1 = pack8<1>(s);
    o0 = MFMA(vf[0][0], p0, o0); o1 = MFMA(vf[1][0], p0, o1); o2 = MFMA(vf[2][0], p0, o2); o3 = MFMA(vf[3][0], p0, o3);
    o0 = MFMA(vf[0][1], p1, o0); o1 = MFMA(vf[1][1], p1, o1); o2 = MFMA(vf[2][1], p1, o2); o3 = MFMA(vf[3][1], p1, o3);
  };
  {
    unsigned slot = 0, fill = 4;
    for (unsigned t = 0; t < NT - 4; ++t) {
      wait_bar<12>();
      issueKV(t + 4, fill);
      step(slot);
      fill = slot;
      slot = (slot == 4) ? 0 : slot + 1;
    }
  }
  wait_bar<12>(); step((NT - 4) % 5);
  wait_bar<8>();  step((NT - 3) % 5);
  wait_bar<4>();  step((NT - 2) % 5);
  wait_bar<0>();  step((NT - 1) % 5);

  l = swap_sum(l);
  {
    const unsigned xw = lds0 + 2 * SLOT + wid * 8192 + lane * 16;
    const f32x16 sa = g ? o0 : o2, sb = g ? o1 : o3;
#pragma unroll
    for (int i = 0; i < 4; ++i) {
      *(LAS f32x4*)(uintptr_t)(xw + i * 1024) = (f32x4){sa[4 * i], sa[4 * i + 1], sa[4 * i + 2], sa[4 * i + 3]};
      *(LAS f32x4*)(uintptr_t)(xw + 4096 + i * 1024) = (f32x4){sb[4 * i], sb[4 * i + 1], sb[4 * i + 2], sb[4 * i + 3]};
    }
    *(LAS f32x2*)(uintptr_t)(lds0 + 4 * SLOT + wid * 512 + lane * 8) = (f32x2){m, l};
    wait_bar<0>();
    const unsigned pw = wid ^ 4;
    const f32x2 ml = *(const LAS f32x2*)(uintptr_t)(lds0 + 4 * SLOT + pw * 512 + lane * 8);
    const float mf = fmaxf(m, ml[0]);
    const float a0 = __builtin_amdgcn_exp2f((m - mf) * CEXP), a1 = __builtin_amdgcn_exp2f((ml[0] - mf) * CEXP);
    const float inv = 1.0f / (a0 * l + a1 * ml[1]);
    const float c0 = a0 * inv, c1 = a1 * inv;
    f32x16 ea = g ? o2 : o0, eb = g ? o3 : o1;
    const unsigned xr = lds0 + 2 * SLOT + pw * 8192 + lane * 16;
#pragma unroll
    for (int i = 0; i < 4; ++i) {
      const f32x4 ua = *(const LAS f32x4*)(uintptr_t)(xr + i * 1024);
      const f32x4 ub = *(const LAS f32x4*)(uintptr_t)(xr + 4096 + i * 1024);
#pragma unroll
      for (int e = 0; e < 4; ++e) {
        ea[4 * i + e] = ea[4 * i + e] * c0 + ua[e] * c1;
        eb[4 * i + e] = eb[4 * i + e] * c0 + ub[e] * c1;
      }
    }
    uint16_t* op = ofrag + ((size_t)(((b * NH + h) * 4 + r) * 8 + 4 * g) * 512 + lane * 8);
    *(bf16x8*)(op) = pack8<0>(ea);
    *(bf16x8*)(op + 512) = pack8<1>(ea);
    *(bf16x8*)(op + 1024) = pack8<0>(eb);
    *(bf16x8*)(op + 1536) = pack8<1>(eb);
  }
}

__global__ __launch_bounds__(512, 2) void oproj_kernel(const uint16_t* __restrict__ ofrag, const uint16_t* __restrict__ wo,
                                                       uint16_t* __restrict__ out) {
  extern __shared__ __attribute__((aligned(16))) char smem[];
  const unsigned tid = threadIdx.x, lane = tid & 63;
  const unsigned wid = rfl(tid >> 6);
  const unsigned l32 = lane & 31, hi = lane >> 5;
  const unsigned bid = blockIdx.x, xcd = bid & 7, idx = bid >> 3;
  const unsigned tm = 4 * (xcd & 3) + (idx & 3), tn = 8 * (xcd >> 2) + (idx >> 2);
  const unsigned b = tm >> 1, qb0 = 2 * (tm & 1);
  const unsigned lds0 = rfl((unsigned)(uintptr_t)smem);
  const unsigned ring = lds0 + wid * 20480;

  const rsrc_t rsW = mkrsrc(wo + (size_t)wid * 512 * DM + tn * 64, 512 * DM * 2 - tn * 128);
  const rsrc_t rsO = mkrsrc(ofrag + (size_t)(b * NH + 4 * wid) * 4 * 8 * 512, 4 * 4 * 8 * 1024);
  const unsigned rw = lane >> 3;
  const unsigned voffW = rw * 2048 + 16 * ((lane & 7) ^ (((rw >> 1) & 1) << 2));
  const unsigned voffO = lane * 16 + qb0 * 8192;
  auto issue = [&](unsigned c, unsigned slot) {
    const unsigned d = ring + slot * 4096;
    dma<0>(d, voffW, rsW, c * 16 * 2048);
    dma<0>(d + 1024, voffW, rsW, c * 16 * 2048 + 8 * 2048);
    const unsigned so = (c >> 3) * 32768 + (c & 7) * 1024;
    dma<0>(d + 2048, voffO, rsO, so);
    dma<0>(d + 3072, voffO, rsO, so + 8192);
  };
  asm volatile("s_nop 4" ::: "memory");
  issue(0, 0); issue(1, 1); issue(2, 2); issue(3, 3);

  const unsigned q4 = (lane & 15) >> 2, p4 = lane & 3, blk = (lane >> 4) & 1;
  const unsigned baseA = ring + 128 * (4 * hi + q4) + 16 * (((q4 >> 1) << 2) + 2 * blk + (p4 >> 1)) + 8 * (p4 & 1);
  const unsigned baseB = ring + 2048 + lane * 16;
  f32x16 a00 = {}, a01 = {}, a10 = {}, a11 = {};
  auto comp = [&](unsigned slot) {
    const unsigned sa = baseA + slot * 4096, sb = baseB + slot * 4096;
    const bf16x8 w0 = cat(lds_tr(sa), lds_tr(sa + 1024));
    const bf16x8 w1 = cat(lds_tr(sa ^ 64), lds_tr((sa ^ 64) + 1024));
    const bf16x8 b0 = lds_b128(sb), b1 = lds_b128(sb + 1024);
    a00 = MFMA(w0, b0, a00); a01 = MFMA(w0, b1, a01);
    a10 = MFMA(w1, b0, a10); a11 = MFMA(w1, b1, a11);
  };
  {
    unsigned slot = 0, fill = 4;
    for (unsigned c = 0; c < 28; ++c) {
      wait_vm<12>();
      issue(c + 4, fill);
      comp(slot);
      fill = slot;
      slot = (slot == 4) ? 0 : slot + 1;
    }
  }
  wait_vm<12>(); comp(28 % 5);
  wait_vm<8>();  comp(29 % 5);
  wait_vm<4>();  comp(30 % 5);
  wait_vm<0>();  comp(31 % 5);
  wait_vm<0>();
  {
    const unsigned xw = ring + lane * 16;
#pragma unroll
    for (int i = 0; i < 4; ++i) {
      *(LAS f32x4*)(uintptr_t)(xw + i * 1024) = (f32x4){a00[4 * i], a00[4 * i + 1], a00[4 * i + 2], a00[4 * i + 3]};
      *(LAS f32x4*)(uintptr_t)(xw + 4096 + i * 1024) = (f32x4){a01[4 * i], a01[4 * i + 1], a01[4 * i + 2], a01[4 * i + 3]};
      *(LAS f32x4*)(uintptr_t)(xw + 8192 + i * 1024) = (f32x4){a10[4 * i], a10[4 * i + 1], a10[4 * i + 2], a10[4 * i + 3]};
      *(LAS f32x4*)(uintptr_t)(xw + 12288 + i * 1024) = (f32x4){a11[4 * i], a11[4 * i + 1], a11[4 * i + 2], a11[4 * i + 3]};
    }
    wait_bar<0>();
    const unsigned xr = lds0 + wid * 2048 + lane * 16;
    f32x4 u0 = {}, u1 = {};
#pragma unroll
    for (int w = 0; w < 8; ++w) {
      u0 += *(const LAS f32x4*)(uintptr_t)(xr + w * 20480);
      u1 += *(const LAS f32x4*)(uintptr_t)(xr + w * 20480 + 1024);
    }
    const unsigned nb = wid >> 2, qb = (wid >> 1) & 1;
    unsigned x0 = cvtpk(u0[0], u0[1]), x1 = cvtpk(u0[2], u0[3]), y0 = cvtpk(u1[0], u1[1]), y1 = cvtpk(u1[2], u1[3]);
    auto r0 = __builtin_amdgcn_permlane32_swap(x0, y0, false, false);
    auto r1 = __builtin_amdgcn_permlane32_swap(x1, y1, false, false);
    const u32x4 w4 = {r0[0], r1[0], r0[1], r1[1]};
    uint16_t* op = out + (size_t)(tm * 64 + qb * 32 + l32) * DM + tn * 64 + nb * 32 + 16 * (wid & 1) + 8 * hi;
    *(u32x4*)op = w4;
  }
}

extern "C" void kernel_launch(void* const* d_in, const int* in_sizes, int n_in, void* d_out, int out_size, void* d_ws,
                              size_t ws_size, hipStream_t stream) {
  const uint16_t* x = (const uint16_t*)d_in[0];
  const uint16_t* kc = (const uint16_t*)d_in[1];
  const uint16_t* vc = (const uint16_t*)d_in[2];
  const uint16_t* wq = (const uint16_t*)d_in[3];
  const uint16_t* wo = (const uint16_t*)d_in[4];
  uint16_t* ofrag = (uint16_t*)d_ws;
  (void)hipFuncSetAttribute((const void*)attn_kernel, hipFuncAttributeMaxDynamicSharedMemorySize, LDS1);
  (void)hipFuncSetAttribute((const void*)oproj_kernel, hipFuncAttributeMaxDynamicSharedMemorySize, LDS2);
  hipLaunchKernelGGL(attn_kernel, dim3(256), dim3(512), LDS1, stream, x, kc, vc, wq, ofrag);
  hipLaunchKernelGGL(oproj_kernel, dim3(256), dim3(512), LDS2, stream, (const uint16_t*)ofrag, wo, (uint16_t*)d_out);
}
```

```cpp
#include <hip/hip_runtime.h>
#include <hip/hip_bf16.h>
#include <stdint.h>
#include <utility>
#include <type_traits>

typedef float f32x16 __attribute__((ext_vector_type(16)));
typedef float f32x4 __attribute__((ext_vector_type(4)));
typedef float f32x2 __attribute__((ext_vector_type(2)));
typedef __bf16 bf16x8 __attribute__((ext_vector_type(8)));
typedef __bf16 bf16x2 __attribute__((ext_vector_type(2)));
typedef short s16x4 __attribute__((ext_vector_type(4)));
typedef short s16x8 __attribute__((ext_vector_type(8)));
typedef unsigned u32x4 __attribute__((ext_vector_type(4)));
typedef unsigned u32x2 __attribute__((ext_vector_type(2)));
typedef __amdgpu_buffer_rsrc_t rsrc_t;

#define LAS __attribute__((address_space(3)))
#define DI __device__ __forceinline__
#define MFMA(a, b, c) __builtin_amdgcn_mfma_f32_32x32x16_bf16((a), (b), (c), 0, 0, 0)
#define SBAR() __builtin_amdgcn_sched_barrier(0)

constexpr int NB = 8, SQ = 128, DM = 1024, NH = 32, SK = 2048, DH = 128;
constexpr int NT = SK / 64;
constexpr unsigned SLOT = 32768;
constexpr int LDS1 = 5 * 32768;
constexpr int LDS2 = 5 * 32768;
constexpr float CEXP = 0.12751743082459868f;
#define THR 8.0f
constexpr float THR_RAW = THR / CEXP;
#define DIST 2
#define PRIO 0
#define XPOL 0
#define WPOL 0

DI unsigned rfl(unsigned x) { return __builtin_amdgcn_readfirstlane(x); }
DI rsrc_t mkrsrc(const void* p, unsigned bytes) {
  return __builtin_amdgcn_make_buffer_rsrc((void*)p, (short)0, (int)bytes, 0x00020000);
}
template <int NTF>
DI void dma(unsigned dst, unsigned voff, rsrc_t rs, unsigned soff) {
  if constexpr (NTF)
    asm volatile("s_mov_b32 m0, %0\n\ts_nop 0\n\tbuffer_load_dwordx4 %1, %2, %3 offen nt lds" ::"s"(dst), "v"(voff), "s"(rs), "s"(soff) : "memory");
  else
    asm volatile("s_mov_b32 m0, %0\n\ts_nop 0\n\tbuffer_load_dwordx4 %1, %2, %3 offen lds" ::"s"(dst), "v"(voff), "s"(rs), "s"(soff) : "memory");
}
template <int N>
DI void wait_bar() { asm volatile("s_waitcnt vmcnt(%0) lgkmcnt(0)\n\ts_barrier" ::"i"(N) : "memory"); }
template <int N>
DI void wait_vm() { asm volatile("s_waitcnt vmcnt(%0) lgkmcnt(0)" ::"i"(N) : "memory"); }

DI bf16x8 lds_b128(unsigned addr) { return *(const LAS bf16x8*)(uintptr_t)addr; }
DI s16x4 lds_tr(unsigned addr) { return __builtin_amdgcn_ds_read_tr16_b64_v4i16((LAS s16x4*)(uintptr_t)addr); }
DI bf16x8 cat(s16x4 a, s16x4 b) { return __builtin_bit_cast(bf16x8, __builtin_shufflevector(a, b, 0, 1, 2, 3, 4, 5, 6, 7)); }
DI unsigned cvtpk(float lo, float hi) {
  f32x2 v = {lo, hi};
  return __builtin_bit_cast(unsigned, __builtin_convertvector(v, bf16x2));
}
template <int S>
DI bf16x8 pack8(const f32x16& v) {
  u32x4 w = {cvtpk(v[8 * S + 0], v[8 * S + 1]), cvtpk(v[8 * S + 2], v[8 * S + 3]), cvtpk(v[8 * S + 4], v[8 * S + 5]), cvtpk(v[8 * S + 6], v[8 * S + 7])};
  return __builtin_bit_cast(bf16x8, w);
}
DI float swap_max(float v) {
  auto rr = __builtin_amdgcn_permlane32_swap(__float_as_uint(v), __float_as_uint(v), false, false);
  return fmaxf(__uint_as_float(rr[0]), __uint_as_float(rr[1]));
}
DI float swap_sum(float v) {
  auto rr = __builtin_amdgcn_permlane32_swap(__float_as_uint(v), __float_as_uint(v), false, false);
  return __uint_as_float(rr[0]) + __uint_as_float(rr[1]);
}

#define STAMP_DECL
#define STAMP(i)
#define STAMP_OUT(base, n)
#define ABL_HNOMM 0
#define ABL_HNORD 0
#define ABL_HNODMA 0
#define CODEPF 0
#define CODEBYTES 20480
#define PIN(x) asm volatile("" : "+v"(x))
#define FOLD 1
#define PRIOAB 1
#define TMASK_A 0x11u
#define TMASK_B 0x44u
#define ISSK 0xFFFFFFFFu
#define ISSV 0xFFFFFFFCu
#define ABL_NODMA 0
#define HT0 0
#define HT1 3
#define HT2 5
#define HT3 8
#define HT4 10
#define HOFF 1
#define SUMLIM 1024.0f
constexpr int hsite(int G, int par, int it) {
  constexpr int HT[5] = {HT0, HT1, HT2, HT3, HT4};
  for (int k = 0; k < 5; ++k) if (HT[k] + par * HOFF + G == it) return k;
  return -1;
}
constexpr int hyoung(int G, int par, int id, int upto) {
  int seq[64] = {}, n = 0;
  seq[n++] = 0; seq[n++] = 1;
  for (int it = -1; it < upto; ++it) {
    const int k = hsite(G, par, it);
    if (G && k >= 0) seq[n++] = 16 + k;
    if (it + 3 <= 15) seq[n++] = it + 3;
    if (!G && k >= 0) seq[n++] = 16 + k;
  }
  for (int i = 0; i < n; ++i) if (seq[i] == id) return n - 1 - i;
  return -1000;
}
static_assert(HT4 + HOFF + 1 <= 14 && HT2 + HOFF + 1 <= 12, "head schedule");
struct Sched { unsigned iss, w3, w2, w1; };
constexpr Sched mksched(unsigned want, int G) {
  Sched s{0, 0, 0, 0};
  int nxt = 5;
  for (int st = 0; st <= NT - 2; ++st) {
    const int lim = G ? st + 3 : st + 5;
    const int need = G ? st : st + 2;
    const int lastneed = G ? (st == NT - 2 ? NT - 1 : st) : st + 2;
    if (nxt < NT && nxt <= lim && (((want >> st) & 1) || nxt <= lastneed)) { s.iss |= 1u << st; ++nxt; }
    int y = nxt - need - 1;
    if (need >= NT || need < 5 || (G && st == NT - 2)) y = 3;
    if (y >= 3) s.w3 |= 1u << st; else if (y == 2) s.w2 |= 1u << st; else if (y == 1) s.w1 |= 1u << st;
  }
  return s;
}
constexpr int schedcount(unsigned m) { int c = 0; for (int i = 0; i < 32; ++i) c += (m >> i) & 1; return c; }
static_assert(schedcount(mksched(ISSK, 0).iss) == NT - 5 && schedcount(mksched(ISSV, 1).iss) == NT - 5, "schedule must issue every tile");

template <int NTF>
DI void dma4(unsigned dst, unsigned va, unsigned vb, unsigned vc_, unsigned vd, rsrc_t rs, unsigned s0, unsigned s1, unsigned s2, unsigned s3) {
  if constexpr (NTF)
    asm volatile("s_mov_b32 m0, %0\n\ts_nop 0\n\t"
                 "buffer_load_dwordx4 %1, %5, %6 offen nt lds\n\t"
                 "buffer_load_dwordx4 %2, %5, %7 offen offset:1024 nt lds\n\t"
                 "buffer_load_dwordx4 %3, %5, %8 offen offset:2048 nt lds\n\t"
                 "buffer_load_dwordx4 %4, %5, %9 offen offset:3072 nt lds"
                 ::"s"(dst), "v"(va), "v"(vb), "v"(vc_), "v"(vd), "s"(rs), "s"(s0), "s"(s1), "s"(s2), "s"(s3) : "memory");
  else
    asm volatile("s_mov_b32 m0, %0\n\ts_nop 0\n\t"
                 "buffer_load_dwordx4 %1, %5, %6 offen lds\n\t"
                 "buffer_load_dwordx4 %2, %5, %7 offen offset:1024 lds\n\t"
                 "buffer_load_dwordx4 %3, %5, %8 offen offset:2048 lds\n\t"
                 "buffer_load_dwordx4 %4, %5, %9 offen offset:3072 lds"
                 ::"s"(dst), "v"(va), "v"(vb), "v"(vc_), "v"(vd), "s"(rs), "s"(s0), "s"(s1), "s"(s2), "s"(s3) : "memory");
}
DI void dma4_if(unsigned mask, unsigned st, unsigned dst, unsigned va, unsigned vb, rsrc_t rs, unsigned so) {
  asm volatile("s_bitcmp1_b32 %0, %1\n\ts_cbranch_scc0 1f\n\t"
               "s_mov_b32 m0, %2\n\ts_nop 0\n\t"
               "buffer_load_dwordx4 %3, %5, %6 offen nt lds\n\t"
               "buffer_load_dwordx4 %3, %5, %6 offen offset:1024 nt lds\n\t"
               "buffer_load_dwordx4 %4, %5, %6 offen offset:2048 nt lds\n\t"
               "buffer_load_dwordx4 %4, %5, %6 offen offset:3072 nt lds\n"
               "1:"
               ::"s"(mask), "s"(st), "s"(dst), "v"(va), "v"(vb), "s"(rs), "s"(so) : "memory", "scc");
}
DI void wait_sched_bar(unsigned w3, unsigned w2, unsigned w1, unsigned st) {
  asm volatile("s_waitcnt vmcnt(12)\n\t"
               "s_bitcmp1_b32 %0, %3\n\ts_cbranch_scc1 1f\n\t"
               "s_waitcnt vmcnt(8)\n\t"
               "s_bitcmp1_b32 %1, %3\n\ts_cbranch_scc1 1f\n\t"
               "s_waitcnt vmcnt(4)\n\t"
               "s_bitcmp1_b32 %2, %3\n\ts_cbranch_scc1 1f\n\t"
               "s_waitcnt vmcnt(0)\n"
               "1:\n\ts_waitcnt lgkmcnt(0)\n\ts_barrier"
               ::"s"(w3), "s"(w2), "s"(w1), "s"(st) : "memory", "scc");
}
DI void ld4_if(unsigned mask, unsigned st, u32x4 (&d)[4], unsigned va, unsigned vb, rsrc_t rs, unsigned so) {
  asm volatile("s_bitcmp1_b32 %4, %5\n\ts_cbranch_scc0 1f\n\t"
               "buffer_load_dwordx4 %0, %6, %8, %9 offen nt\n\t"
               "buffer_load_dwordx4 %1, %6, %8, %9 offen offset:1024 nt\n\t"
               "buffer_load_dwordx4 %2, %7, %8, %9 offen offset:2048 nt\n\t"
               "buffer_load_dwordx4 %3, %7, %8, %9 offen offset:3072 nt\n"
               "1:"
               : "+v"(d[0]), "+v"(d[1]), "+v"(d[2]), "+v"(d[3])
               : "s"(mask), "s"(st), "v"(va), "v"(vb), "s"(rs), "s"(so) : "memory", "scc");
}
template <int N0, int N1>
DI void wait_par(unsigned par) {
  if constexpr (N0 == N1) asm volatile("s_waitcnt vmcnt(%0)" ::"i"(N0) : "memory");
  else asm volatile("s_waitcnt vmcnt(%0)\n\ts_cmp_eq_u32 %2, %3\n\ts_cbranch_scc1 1f\n\ts_waitcnt vmcnt(%1)\n1:"
                    ::"i"(N0 > N1 ? N0 : N1), "i"(N0 > N1 ? N1 : N0), "s"(par), "i"(N0 > N1 ? 0 : 1) : "memory", "scc");
}
template <class F, int... Is>
DI void sfor_impl(F&& f, std::integer_sequence<int, Is...>) { (f(std::integral_constant<int, Is>{}), ...); }
template <int N, class F>
DI void sfor(F&& f) { sfor_impl(f, std::make_integer_sequence<int, N>{}); }
DI void lbar() { asm volatile("s_waitcnt lgkmcnt(0)\n\ts_barrier" ::: "memory"); }
struct QFrag { bf16x8 xb[4]; s16x4 wa[4][2], wb[4][2]; };

#define CT(x)
#define CH(x)

#define IT(i)
template <int G>
DI void attn_body(const uint16_t* __restrict__ x, const uint16_t* __restrict__ kc, const uint16_t* __restrict__ vc,
                  const uint16_t* __restrict__ wq, uint16_t* __restrict__ ofrag, unsigned lds0, unsigned r, unsigned long long pc) {
  constexpr unsigned g = G;
  const unsigned lane = threadIdx.x & 63, wid = 4 * g + r;
  const unsigned l32 = lane & 31, hi = lane >> 5;
  const unsigned bid = blockIdx.x, xcd = bid & 7, idx = bid >> 3;
  const unsigned h = 4 * xcd + (idx & 3), b = idx >> 2;
  STAMP_DECL
  unsigned itr_ = 0, itc_ = 0;
  STAMP(0);
  IT(0);

  const unsigned l4 = lane >> 4;
  const size_t kvoff = (size_t)(b * NH + h) * SK * DH;
  const rsrc_t rsKV = mkrsrc((g ? vc : kc) + kvoff, SK * DH * 2);
  const rsrc_t rsQ = g ? mkrsrc(wq + h * DH, DM * NH * DH * 2 - h * DH * 2) : mkrsrc(x + (size_t)b * SQ * DM, SQ * DM * 2);
  const unsigned voffA = (16 * r + l4) * 256 + 16 * ((lane & 15) ^ (g ? (l4 << 2) : (l4 | ((r & 1) << 3))));
  const unsigned voffB = g ? voffA : voffA ^ 64;
  const unsigned voffQ = g ? (16 * r + l4) * 8192 + 16 * ((lane & 15) ^ (l4 << 2))
                           : (32 * r + (lane >> 3)) * 2048 + 16 * ((lane & 7) ^ l4);
  const unsigned voffQ1 = g ? voffQ : voffQ ^ 64;
  constexpr unsigned qstS = g ? 64 * 8192 : 128, qstJ = (g ? 4 * 8192 : 8 * 2048) - 1024;
  const unsigned dstw = lds0 + g * 16384 + r * 4096;
  const unsigned kstart = (2 * b + 4 * (h & 3)) & 15;

  auto issueQ = [&](unsigned i, unsigned so_) {
    const unsigned so = ((i + kstart) & 15) * qstS;
    dma4<g ? WPOL : XPOL>(dstw + so_, voffQ, voffQ1, voffQ, voffQ1, rsQ, so, so + qstJ, so + 2 * qstJ, so + 3 * qstJ);
  };
  auto issueKV = [&](unsigned t, unsigned so_) {
    const unsigned so = t * 16384;
    dma4<1>(dstw + so_, voffA, voffA, voffB, voffB, rsKV, so, so, so, so);
  };

  asm volatile("s_nop 4" ::: "memory");
  issueQ(0, 4 * SLOT); issueQ(1, 2 * SLOT);

  const unsigned q4 = (lane & 15) >> 2, p4 = lane & 3, blk = (lane >> 4) & 1;
  const unsigned xrow = 32 * r + l32;
  const unsigned baseX = lds0 + 128 * xrow + 16 * (hi ^ ((xrow >> 1) & 7));
  const unsigned baseW = lds0 + 16384 + 256 * (8 * hi + q4) + 16 * (4 * ((2 * g) ^ q4) + 2 * blk + (p4 & 1)) + 8 * (p4 >> 1);

  const unsigned baseK = lds0 + 8192 * g + 256 * l32 + 16 * (hi ^ ((l32 & 3) | ((l32 >> 3) << 2)));
  const unsigned baseV = lds0 + 16384 + 8192 * g + 256 * (4 * hi + q4) + 16 * (4 * q4 + 2 * blk + (p4 >> 1)) + 8 * (p4 & 1);

  f32x16 qa0 = {}, qa1 = {};
  QFrag fr[2];
  u32x4 stg[3][4];
#pragma unroll
  for (int k = 0; k < 3; ++k) asm volatile("" : "=v"(stg[k][0]), "=v"(stg[k][1]), "=v"(stg[k][2]), "=v"(stg[k][3]));
  const unsigned par = idx & 1;
  auto qiter = [&](QFrag& cur, QFrag& nxt, unsigned so_, bool mm) {
    unsigned sx = baseX + so_, sw0 = baseW + so_;
    PIN(sx); PIN(sw0);
    const unsigned sw1 = sw0 ^ 64;
#pragma unroll
    for (int s = 0; s < 4; ++s) {
      nxt.xb[s] = lds_b128(sx ^ (s << 5)); nxt.wa[s][0] = lds_tr(sw0 + 4096 * s); nxt.wa[s][1] = lds_tr(sw0 + 4096 * s + 1024);
      SBAR();
      if (mm) { qa0 = MFMA(cat(cur.wa[s][0], cur.wa[s][1]), cur.xb[s], qa0); PIN(qa0); }
      SBAR();
      nxt.wb[s][0] = lds_tr(sw1 + 4096 * s); nxt.wb[s][1] = lds_tr(sw1 + 4096 * s + 1024);
      SBAR();
      if (mm) { qa1 = MFMA(cat(cur.wb[s][0], cur.wb[s][1]), cur.xb[s], qa1); PIN(qa1); }
      SBAR();
    }
  };
  auto tile_req = [&](auto kc_, unsigned pm) {
    constexpr int k = decltype(kc_)::value;
    if constexpr (k < 2) dma4_if(pm, par, dstw + k * SLOT, voffA, voffB, rsKV, k * 16384);
    else ld4_if(pm, par, stg[k - 2], voffA, voffB, rsKV, k * 16384);
  };
  auto hslot = [&](auto ic) {
    constexpr int it = decltype(ic)::value - 1;
    constexpr int k0 = hsite(G, 0, it), k1 = hsite(G, 1, it);
    if constexpr (!G && it + 3 <= 15) issueQ(it + 3, (2 + (it + 5) % 3) * SLOT);
    if constexpr (k0 >= 0 && k0 == k1) tile_req(std::integral_constant<int, k0>{}, 3);
    else {
      if constexpr (k0 >= 0) tile_req(std::integral_constant<int, k0>{}, 1);
      if constexpr (k1 >= 0) tile_req(std::integral_constant<int, k1>{}, 2);
    }
    if constexpr (G && it + 3 <= 15) issueQ(it + 3, (2 + (it + 5) % 3) * SLOT);
  };
  auto stg_write = [&](auto kc_, auto uptoc) {
    constexpr int k = decltype(kc_)::value, upto = decltype(uptoc)::value;
    wait_par<4 * hyoung(G, 0, 16 + k, upto), 4 * hyoung(G, 1, 16 + k, upto)>(par);
    asm volatile("" : "+v"(stg[k - 2][0]), "+v"(stg[k - 2][1]), "+v"(stg[k - 2][2]), "+v"(stg[k - 2][3]));
#pragma unroll
    for (int j = 0; j < 4; ++j) *(LAS u32x4*)(uintptr_t)(dstw + k * SLOT + j * 1024 + lane * 16) = stg[k - 2][j];
  };
  STAMP(1);
  wait_bar<4>();
  STAMP(2);
  IT(1);
  qiter(fr[1], fr[0], 4 * SLOT, false);
  hslot(std::integral_constant<int, 0>{});
  sfor<15>([&](auto ic) {
    constexpr int i = decltype(ic)::value;
    wait_par<4 * hyoung(G, 0, i + 1, i), 4 * hyoung(G, 1, i + 1, i)>(par);
    lbar();
    IT(i + 2);
    if constexpr (G) hslot(std::integral_constant<int, i + 1>{});
    if constexpr (G && i == 13) stg_write(std::integral_constant<int, 2>{}, std::integral_constant<int, 14>{});
    qiter(fr[i & 1], fr[(i + 1) & 1], (2 + (i + 3) % 3) * SLOT, true);
    if constexpr (!G) hslot(std::integral_constant<int, i + 1>{});
    if constexpr (!G && i == 13) stg_write(std::integral_constant<int, 2>{}, std::integral_constant<int, 14>{});
  });
  STAMP(3);
  bf16x8 qf[8], kf[8];
  {
    static_assert(hyoung(G, 0, 17, 14) >= hyoung(G, 0, 15, 14) && hyoung(G, 1, 17, 14) >= hyoung(G, 1, 15, 14), "tiles 0, 1 land before stage 15");
    QFrag& cur = fr[1];
    const unsigned xw = lds0 + 3 * SLOT + wid * 4096 + lane * 16;
#pragma unroll
    for (int s = 0; s < 4; ++s) {
      kf[2 * s] = lds_b128(baseK ^ (s << 6)); kf[2 * s + 1] = lds_b128(baseK ^ ((2 * s + 1) << 5));
      SBAR();
      qa0 = MFMA(cat(cur.wa[s][0], cur.wa[s][1]), cur.xb[s], qa0); PIN(qa0);
      SBAR();
    }
    qa1 = MFMA(cat(cur.wb[0][0], cur.wb[0][1]), cur.xb[0], qa1);
    qa1 = MFMA(cat(cur.wb[1][0], cur.wb[1][1]), cur.xb[1], qa1); PIN(qa1);
    SBAR();
    if (FOLD) qa0 *= CEXP;
    qf[4 * g + 0] = pack8<0>(qa0); PIN(qf[4 * g + 0]);
    SBAR();
    qa1 = MFMA(cat(cur.wb[2][0], cur.wb[2][1]), cur.xb[2], qa1); PIN(qa1);
    SBAR();
    qf[4 * g + 1] = pack8<1>(qa0); PIN(qf[4 * g + 1]);
    SBAR();
    qa1 = MFMA(cat(cur.wb[3][0], cur.wb[3][1]), cur.xb[3], qa1); PIN(qa1);
    SBAR();
    *(LAS bf16x8*)(uintptr_t)(xw) = qf[4 * g + 0];
    *(LAS bf16x8*)(uintptr_t)(xw + 1024) = qf[4 * g + 1];
    if (FOLD) qa1 *= CEXP;
    qf[4 * g + 2] = pack8<0>(qa1); qf[4 * g + 3] = pack8<1>(qa1);
    *(LAS bf16x8*)(uintptr_t)(xw + 2048) = qf[4 * g + 2];
    *(LAS bf16x8*)(uintptr_t)(xw + 3072) = qf[4 * g + 3];
    lbar();
    const unsigned xr = lds0 + 3 * SLOT + (wid ^ 4) * 4096 + lane * 16;
#pragma unroll
    for (int f = 0; f < 4; ++f) qf[4 * (1 - g) + f] = lds_b128(xr + f * 1024);
  }
  STAMP(4);
  IT(17);

  f32x16 o0, o1, o2, o3, sc, negm;
  s16x4 va[8], vb[8];
  u32x4 pa0, pa1, pb0, pb1;
  float m, l;

  auto rowmax = [&](const f32x16& s) {
    float a = fmaxf(fmaxf(s[0], s[1]), s[2]), c = fmaxf(fmaxf(s[3], s[4]), s[5]);
    a = fmaxf(fmaxf(a, s[6]), s[7]); c = fmaxf(fmaxf(c, s[8]), s[9]);
    a = fmaxf(fmaxf(a, s[10]), s[11]); c = fmaxf(fmaxf(c, s[12]), s[13]);
    a = fmaxf(fmaxf(a, s[14]), s[15]);
    return swap_max(fmaxf(a, c));
  };
#define EX(v) __builtin_amdgcn_exp2f(v)
  auto probs = [&](u32x4& p0, u32x4& p1) {
    const float nm = -m;
    float sacc = 0.f;
#pragma unroll
    for (int i = 0; i < 8; ++i) {
      const float e0 = EX(sc[2 * i]), e1 = EX(sc[2 * i + 1]);
      sacc += e0; sacc += e1;
      if (i < 4) p0[i] = cvtpk(e0, e1); else p1[i - 4] = cvtpk(e0, e1);
    }
    return sacc;
  };
  auto guard = [&](float sacc, u32x4& p0, u32x4& p1) {
    if (__builtin_expect(__any(!(sacc <= SUMLIM)), 0)) {
      const float rm = FOLD ? rowmax(sc) : __builtin_fmaf(rowmax(sc), CEXP, -m);
      const float dl = fmaxf(rm, 0.f);
      const float alpha = __builtin_amdgcn_exp2f(-dl);
      m += dl;
      l *= alpha;
      o0 *= alpha; o1 *= alpha; o2 *= alpha; o3 *= alpha;
      if (FOLD) { sc -= dl; negm -= dl; }
      sacc = probs(p0, p1);
    }
    l += sacc;
  };
#define VRD(i, sv) do { const unsigned a_ = ((sv) ^ (((i) & 3) << 6)) + 4096 * ((i) >> 2); va[i] = lds_tr(a_); vb[i] = lds_tr(a_ + 2048); } while (0)
#define BF(x) __builtin_bit_cast(bf16x8, x)
  auto phaseA = [&](unsigned sv, bool vr) {
    if (PRIOAB) __builtin_amdgcn_s_setprio(PRIOAB == 1 ? 1 : 0);
#pragma unroll
    for (int k = 0; k < 8; ++k) {
      if (vr) VRD(k, sv);
      SBAR();
      sc = MFMA(kf[k], qf[k], k == 0 ? negm : sc); PIN(sc);
      SBAR();
    }
  };
  auto phaseB = [&](const u32x4& po0, const u32x4& po1, u32x4& pn0, u32x4& pn1, unsigned sk) {
    const float nm = -m;
    float sacc = 0.f;
    if (PRIOAB) __builtin_amdgcn_s_setprio(PRIOAB == 1 ? 0 : 1);
    SBAR();
#pragma unroll
    for (int i = 0; i < 8; ++i) {
      if (i < 4) { kf[2 * i] = lds_b128(sk ^ (i << 6)); kf[2 * i + 1] = lds_b128(sk ^ ((2 * i + 1) << 5)); }
      SBAR();
      const bf16x8 vf = cat(va[i], vb[i]);
      const bf16x8 pf = i < 4 ? BF(po0) : BF(po1);
      if ((i & 3) == 0) { o0 = MFMA(vf, pf, o0); PIN(o0); }
      if ((i & 3) == 1) { o1 = MFMA(vf, pf, o1); PIN(o1); }
      if ((i & 3) == 2) { o2 = MFMA(vf, pf, o2); PIN(o2); }
      if ((i & 3) == 3) { o3 = MFMA(vf, pf, o3); PIN(o3); }
      SBAR();
      const float e0 = EX(sc[2 * i]), e1 = EX(sc[2 * i + 1]);
      if (i == 0) sacc = e0 + e1; else { sacc += e0; sacc += e1; }
      if (i < 4) { pn0[i] = cvtpk(e0, e1); PIN(pn0); } else { pn1[i - 4] = cvtpk(e0, e1); PIN(pn1); }
      PIN(sacc);
      SBAR();
    }
    asm volatile("" ::: "memory");
    return sacc;
  };

  constexpr Sched SC = mksched(g ? ISSV : ISSK, g);
  unsigned nso = 5 * 16384, fill = dstw;
  auto kvissue = [&](unsigned st) {
    if (ABL_NODMA) return;
    dma4_if(SC.iss, st, fill, voffA, voffB, rsKV, nso);
    if ((SC.iss >> st) & 1) { nso += 16384; fill = (fill == dstw + 4 * SLOT) ? dstw : fill + SLOT; }
  };
  {
    sc = (f32x16){};
#pragma unroll
    for (int k = 0; k < 4; ++k) sc = MFMA(kf[4 * g + k], qf[4 * g + k], sc);
    PIN(sc);
    SBAR();
    stg_write(std::integral_constant<int, 4>{}, std::integral_constant<int, 15>{});
    if (!g) kvissue(0);
    SBAR();
#pragma unroll
    for (int k = 0; k < 4; ++k) sc = MFMA(kf[4 * (1 - g) + k], qf[4 * (1 - g) + k], sc);
    PIN(sc);
    SBAR();
#pragma unroll
    for (int k = 0; k < 8; ++k) kf[k] = lds_b128((baseK + SLOT) ^ (k << 5));
    o0 = (f32x16){}; o1 = (f32x16){}; o2 = (f32x16){}; o3 = (f32x16){};
    PIN(o0); PIN(o1); PIN(o2); PIN(o3);
    SBAR();
    m = FOLD ? rowmax(sc) : rowmax(sc) * CEXP;
#pragma unroll
    for (int i = 0; i < 16; ++i) { negm[i] = FOLD ? -m : 0.f; if (FOLD) sc[i] -= m; }
    PIN(negm);
    l = probs(pa0, pa1);
    PIN(pa0); PIN(pa1); PIN(l);
    lbar();
    IT(18);
    asm volatile("" : "+v"(stg[1][0]), "+v"(stg[1][1]), "+v"(stg[1][2]), "+v"(stg[1][3]));
#pragma unroll
    for (int j = 0; j < 4; ++j) *(LAS u32x4*)(uintptr_t)(dstw + 3 * SLOT + j * 1024 + lane * 16) = stg[1][j];
  }
  STAMP(5);
  {
    unsigned sok = 2 * SLOT, sov = 0;
    auto step = [&](unsigned j, const u32x4& po0, const u32x4& po1, u32x4& pn0, u32x4& pn1) {
      if (g) kvissue(j);
      phaseA(baseV + sov, true);
      const float sacc = phaseB(po0, po1, pn0, pn1, baseK + sok);
      if (!g) kvissue(j);
      guard(sacc, pn0, pn1);
      sok = (sok == 4 * SLOT) ? 0 : sok + SLOT;
      sov = (sov == 4 * SLOT) ? 0 : sov + SLOT;
      wait_sched_bar(SC.w3, SC.w2, SC.w1, j);
      IT(18 + j);
    };
    for (unsigned j = 1; j < NT - 1; j += 2) { step(j, pa0, pa1, pb0, pb1); step(j + 1, pb0, pb1, pa0, pa1); }
  }
  STAMP(6);
  {
    phaseA(0, false);
    const float sacc = probs(pb0, pb1);
    PIN(pb0); PIN(pb1);
    wait_bar<0>();
    STAMP(7);
    const unsigned sv = baseV + (NT - 2) % 5 * SLOT, su = baseV + (NT - 1) % 5 * SLOT;
#pragma unroll
    for (int i = 0; i < 8; ++i) VRD(i, sv);
    s16x4 ua[8], ub[8];
#pragma unroll
    for (int i = 0; i < 8; ++i) { const unsigned a_ = (su ^ ((i & 3) << 6)) + 4096 * (i >> 2); ua[i] = lds_tr(a_); ub[i] = lds_tr(a_ + 2048); }
    asm volatile("" ::: "memory");
    o0 = MFMA(cat(va[0], vb[0]), BF(pa0), o0); o1 = MFMA(cat(va[1], vb[1]), BF(pa0), o1);
    o2 = MFMA(cat(va[2], vb[2]), BF(pa0), o2); o3 = MFMA(cat(va[3], vb[3]), BF(pa0), o3);
    o0 = MFMA(cat(va[4], vb[4]), BF(pa1), o0); o1 = MFMA(cat(va[5], vb[5]), BF(pa1), o1);
    o2 = MFMA(cat(va[6], vb[6]), BF(pa1), o2); o3 = MFMA(cat(va[7], vb[7]), BF(pa1), o3);
    guard(sacc, pb0, pb1);
    o0 = MFMA(cat(ua[0], ub[0]), BF(pb0), o0); o1 = MFMA(cat(ua[1], ub[1]), BF(pb0), o1);
    o2 = MFMA(cat(ua[2], ub[2]), BF(pb0), o2); o3 = MFMA(cat(ua[3], ub[3]), BF(pb0), o3);
    o0 = MFMA(cat(ua[4], ub[4]), BF(pb1), o0); o1 = MFMA(cat(ua[5], ub[5]), BF(pb1), o1);
    o2 = MFMA(cat(ua[6], ub[6]), BF(pb1), o2); o3 = MFMA(cat(ua[7], ub[7]), BF(pb1), o3);
  }
  STAMP(8);

  l = swap_sum(l);
  {
    const unsigned xw = lds0 + 2 * SLOT + wid * 8192 + lane * 16;
    const f32x16 sa = g ? o0 : o2, sb = g ? o1 : o3;
#pragma unroll
    for (int i = 0; i < 4; ++i) {
      *(LAS f32x4*)(uintptr_t)(xw + i * 1024) = (f32x4){sa[4 * i], sa[4 * i + 1], sa[4 * i + 2], sa[4 * i + 3]};
      *(LAS f32x4*)(uintptr_t)(xw + 4096 + i * 1024) = (f32x4){sb[4 * i], sb[4 * i + 1], sb[4 * i + 2], sb[4 * i + 3]};
    }
    *(LAS f32x2*)(uintptr_t)(lds0 + 4 * SLOT + wid * 512 + lane * 8) = (f32x2){m, l};
    lbar();
    const unsigned pw = wid ^ 4;
    const f32x2 ml = *(const LAS f32x2*)(uintptr_t)(lds0 + 4 * SLOT + pw * 512 + lane * 8);
    const float mf = fmaxf(m, ml[0]);
    const float a0 = __builtin_amdgcn_exp2f(m - mf), a1 = __builtin_amdgcn_exp2f(ml[0] - mf);
    const float inv = 1.0f / (a0 * l + a1 * ml[1]);
    const float c0 = a0 * inv, c1 = a1 * inv;
    f32x16 ea = g ? o2 : o0, eb = g ? o3 : o1;
    const unsigned xr = lds0 + 2 * SLOT + pw * 8192 + lane * 16;
#pragma unroll
    for (int i = 0; i < 4; ++i) {
      const f32x4 ua = *(const LAS f32x4*)(uintptr_t)(xr + i * 1024);
      const f32x4 ub = *(const LAS f32x4*)(uintptr_t)(xr + 4096 + i * 1024);
#pragma unroll
      for (int e = 0; e < 4; ++e) {
        ea[4 * i + e] = ea[4 * i + e] * c0 + ua[e] * c1;
        eb[4 * i + e] = eb[4 * i + e] * c0 + ub[e] * c1;
      }
    }
    uint16_t* op = ofrag + ((size_t)(((b * NH + h) * 4 + r) * 8 + 4 * g) * 512 + lane * 8);
    *(bf16x8*)(op) = pack8<0>(ea);
    *(bf16x8*)(op + 512) = pack8<1>(ea);
    *(bf16x8*)(op + 1024) = pack8<0>(eb);
    *(bf16x8*)(op + 1536) = pack8<1>(eb);
  }
}

__global__ __launch_bounds__(512, 2) void attn_kernel(const uint16_t* __restrict__ x, const uint16_t* __restrict__ kc,
                                                      const uint16_t* __restrict__ vc, const uint16_t* __restrict__ wq,
                                                      uint16_t* __restrict__ ofrag) {
  extern __shared__ __attribute__((aligned(16))) char smem[];
  const unsigned wid = rfl(threadIdx.x >> 6);
  const unsigned lds0 = rfl((unsigned)(uintptr_t)smem);
  unsigned long long pc;
  asm volatile("s_getpc_b64 %0" : "=s"(pc));
  if (wid >> 2) attn_body<1>(x, kc, vc, wq, ofrag, lds0, wid & 3, pc);
  else attn_body<0>(x, kc, vc, wq, ofrag, lds0, wid & 3, pc);
}

__global__ __launch_bounds__(512, 2) void oproj_kernel(const uint16_t* __restrict__ ofrag, const uint16_t* __restrict__ wo,
                                                       uint16_t* __restrict__ out) {
  extern __shared__ __attribute__((aligned(16))) char smem[];
  const unsigned tid = threadIdx.x, lane = tid & 63;
  const unsigned wid = rfl(tid >> 6);
  const unsigned l32 = lane & 31, hi = lane >> 5;
  const unsigned bid = blockIdx.x, xcd = bid & 7, idx = bid >> 3;
  const unsigned tm = 4 * (xcd & 3) + (idx & 3), tn = 8 * (xcd >> 2) + (idx >> 2);
  const unsigned b = tm >> 1, qb0 = 2 * (tm & 1);
  const unsigned lds0 = rfl((unsigned)(uintptr_t)smem);
  STAMP_DECL
  STAMP(0);
  const unsigned ring = lds0 + wid * 20480;

  const rsrc_t rsW = mkrsrc(wo + (size_t)wid * 512 * DM + tn * 64, 512 * DM * 2 - tn * 128);
  const rsrc_t rsO = mkrsrc(ofrag + (size_t)(b * NH + 4 * wid) * 4 * 8 * 512, 4 * 4 * 8 * 1024);
  const unsigned rw = lane >> 3;
  const unsigned voffW = rw * 2048 + 16 * ((lane & 7) ^ (((rw >> 1) & 1) << 2));
  const unsigned voffO = lane * 16 + qb0 * 8192;
  auto issue = [&](unsigned c, unsigned slot) {
    const unsigned d = ring + slot * 4096;
    dma<0>(d, voffW, rsW, c * 16 * 2048);
    dma<0>(d + 1024, voffW, rsW, c * 16 * 2048 + 8 * 2048);
    const unsigned so = (c >> 3) * 32768 + (c & 7) * 1024;
    dma<0>(d + 2048, voffO, rsO, so);
    dma<0>(d + 3072, voffO, rsO, so + 8192);
  };
  asm volatile("s_nop 4" ::: "memory");
  issue(0, 0); issue(1, 1); issue(2, 2); issue(3, 3);

  const unsigned q4 = (lane & 15) >> 2, p4 = lane & 3, blk = (lane >> 4) & 1;
  const unsigned baseA = ring + 128 * (4 * hi + q4) + 16 * (((q4 >> 1) << 2) + 2 * blk + (p4 >> 1)) + 8 * (p4 & 1);
  const unsigned baseB = ring + 2048 + lane * 16;
  f32x16 a00 = {}, a01 = {}, a10 = {}, a11 = {};
  auto comp = [&](unsigned slot) {
    const unsigned sa = baseA + slot * 4096, sb = baseB + slot * 4096;
    const bf16x8 w0 = cat(lds_tr(sa), lds_tr(sa + 1024));
    const bf16x8 w1 = cat(lds_tr(sa ^ 64), lds_tr((sa ^ 64) + 1024));
    const bf16x8 b0 = lds_b128(sb), b1 = lds_b128(sb + 1024);
    a00 = MFMA(w0, b0, a00); a01 = MFMA(w0, b1, a01);
    a10 = MFMA(w1, b0, a10); a11 = MFMA(w1, b1, a11);
  };
  STAMP(1);
  wait_vm<12>();
  STAMP(2);
  {
    unsigned slot = 0, fill = 4;
    for (unsigned c = 0; c < 28; ++c) {
      wait_vm<12>();
      issue(c + 4, fill);
      comp(slot);
      fill = slot;
      slot = (slot == 4) ? 0 : slot + 1;
    }
  }
  wait_vm<12>(); comp(28 % 5);
  wait_vm<8>();  comp(29 % 5);
  wait_vm<4>();  comp(30 % 5);
  wait_vm<0>();  comp(31 % 5);
  wait_vm<0>();
  STAMP(3);
  {
    const unsigned xw = ring + lane * 16;
#pragma unroll
    for (int i = 0; i < 4; ++i) {
      *(LAS f32x4*)(uintptr_t)(xw + i * 1024) = (f32x4){a00[4 * i], a00[4 * i + 1], a00[4 * i + 2], a00[4 * i + 3]};
      *(LAS f32x4*)(uintptr_t)(xw + 4096 + i * 1024) = (f32x4){a01[4 * i], a01[4 * i + 1], a01[4 * i + 2], a01[4 * i + 3]};
      *(LAS f32x4*)(uintptr_t)(xw + 8192 + i * 1024) = (f32x4){a10[4 * i], a10[4 * i + 1], a10[4 * i + 2], a10[4 * i + 3]};
      *(LAS f32x4*)(uintptr_t)(xw + 12288 + i * 1024) = (f32x4){a11[4 * i], a11[4 * i + 1], a11[4 * i + 2], a11[4 * i + 3]};
    }
    wait_bar<0>();
    const unsigned xr = lds0 + wid * 2048 + lane * 16;
    f32x4 u0 = {}, u1 = {};
#pragma unroll
    for (int w = 0; w < 8; ++w) {
      u0 += *(const LAS f32x4*)(uintptr_t)(xr + w * 20480);
      u1 += *(const LAS f32x4*)(uintptr_t)(xr + w * 20480 + 1024);
    }
    const unsigned nb = wid >> 2, qb = (wid >> 1) & 1;
    unsigned x0 = cvtpk(u0[0], u0[1]), x1 = cvtpk(u0[2], u0[3]), y0 = cvtpk(u1[0], u1[1]), y1 = cvtpk(u1[2], u1[3]);
    auto r0 = __builtin_amdgcn_permlane32_swap(x0, y0, false, false);
    auto r1 = __builtin_amdgcn_permlane32_swap(x1, y1, false, false);
    const u32x4 w4 = {r0[0], r1[0], r0[1], r1[1]};
    uint16_t* op = out + (size_t)(tm * 64 + qb * 32 + l32) * DM + tn * 64 + nb * 32 + 16 * (wid & 1) + 8 * hi;
    *(u32x4*)op = w4;
  }
}

extern "C" void kernel_launch(void* const* d_in, const int* in_sizes, int n_in, void* d_out, int out_size, void* d_ws,
                              size_t ws_size, hipStream_t stream) {
  const uint16_t* x = (const uint16_t*)d_in[0];
  const uint16_t* kc = (const uint16_t*)d_in[1];
  const uint16_t* vc = (const uint16_t*)d_in[2];
  const uint16_t* wq = (const uint16_t*)d_in[3];
  const uint16_t* wo = (const uint16_t*)d_in[4];
  uint16_t* ofrag = (uint16_t*)d_ws;
  (void)hipFuncSetAttribute((const void*)attn_kernel, hipFuncAttributeMaxDynamicSharedMemorySize, LDS1);
  (void)hipFuncSetAttribute((const void*)oproj_kernel, hipFuncAttributeMaxDynamicSharedMemorySize, LDS2);
  hipLaunchKernelGGL(attn_kernel, dim3(256), dim3(512), LDS1, stream, x, kc, vc, wq, ofrag);
  hipLaunchKernelGGL(oproj_kernel, dim3(256), dim3(512), LDS2, stream, (const uint16_t*)ofrag, wo, (uint16_t*)d_out);
}
```

```cpp
#include <hip/hip_runtime.h>
#include <hip/hip_bf16.h>
#include <stdint.h>

typedef float f32x16 __attribute__((ext_vector_type(16)));
typedef float f32x4 __attribute__((ext_vector_type(4)));
typedef float f32x2 __attribute__((ext_vector_type(2)));
typedef __bf16 bf16x8 __attribute__((ext_vector_type(8)));
typedef __bf16 bf16x2 __attribute__((ext_vector_type(2)));
typedef short s16x4 __attribute__((ext_vector_type(4)));
typedef short s16x8 __attribute__((ext_vector_type(8)));
typedef unsigned u32x4 __attribute__((ext_vector_type(4)));
typedef unsigned u32x2 __attribute__((ext_vector_type(2)));
typedef __amdgpu_buffer_rsrc_t rsrc_t;

#define LAS __attribute__((address_space(3)))
#define DI __device__ __forceinline__
#define MFMA(a, b, c) __builtin_amdgcn_mfma_f32_32x32x16_bf16((a), (b), (c), 0, 0, 0)
#define SBAR() __builtin_amdgcn_sched_barrier(0)

constexpr int NB = 8, SQ = 128, DM = 1024, NH = 32, SK = 2048, DH = 128;
constexpr int NT = SK / 64;
constexpr unsigned SLOT = 32768;
constexpr int LDS1 = 5 * 32768;
constexpr int LDS2 = 5 * 32768;
constexpr float CEXP = 0.12751743082459868f;

DI unsigned rfl(unsigned x) { return __builtin_amdgcn_readfirstlane(x); }
DI rsrc_t mkrsrc(const void* p, unsigned bytes) {
  return __builtin_amdgcn_make_buffer_rsrc((void*)p, (short)0, (int)bytes, 0x00020000);
}
template <int NTF>
DI void dma(unsigned dst, unsigned voff, rsrc_t rs, unsigned soff) {
  if constexpr (NTF)
    asm volatile("s_mov_b32 m0, %0\n\ts_nop 0\n\tbuffer_load_dwordx4 %1, %2, %3 offen nt lds" ::"s"(dst), "v"(voff), "s"(rs), "s"(soff) : "memory");
  else
    asm volatile("s_mov_b32 m0, %0\n\ts_nop 0\n\tbuffer_load_dwordx4 %1, %2, %3 offen lds" ::"s"(dst), "v"(voff), "s"(rs), "s"(soff) : "memory");
}
template <int N>
DI void wait_bar() { asm volatile("s_waitcnt vmcnt(%0) lgkmcnt(0)\n\ts_barrier" ::"i"(N) : "memory"); }
template <int N>
DI void wait_vm() { asm volatile("s_waitcnt vmcnt(%0) lgkmcnt(0)" ::"i"(N) : "memory"); }

DI bf16x8 lds_b128(unsigned addr) { return *(const LAS bf16x8*)(uintptr_t)addr; }
DI s16x4 lds_tr(unsigned addr) { return __builtin_amdgcn_ds_read_tr16_b64_v4i16((LAS s16x4*)(uintptr_t)addr); }
DI bf16x8 cat(s16x4 a, s16x4 b) { return __builtin_bit_cast(bf16x8, __builtin_shufflevector(a, b, 0, 1, 2, 3, 4, 5, 6, 7)); }
DI unsigned cvtpk(float lo, float hi) {
  f32x2 v = {lo, hi};
  return __builtin_bit_cast(unsigned, __builtin_convertvector(v, bf16x2));
}
template <int S>
DI bf16x8 pack8(const f32x16& v) {
  u32x4 w = {cvtpk(v[8 * S + 0], v[8 * S + 1]), cvtpk(v[8 * S + 2], v[8 * S + 3]), cvtpk(v[8 * S + 4], v[8 * S + 5]), cvtpk(v[8 * S + 6], v[8 * S + 7])};
  return __builtin_bit_cast(bf16x8, w);
}
DI float swap_max(float v) {
  auto rr = __builtin_amdgcn_permlane32_swap(__float_as_uint(v), __float_as_uint(v), false, false);
  return fmaxf(__uint_as_float(rr[0]), __uint_as_float(rr[1]));
}
DI float swap_sum(float v) {
  auto rr = __builtin_amdgcn_permlane32_swap(__float_as_uint(v), __float_as_uint(v), false, false);
  return __uint_as_float(rr[0]) + __uint_as_float(rr[1]);
}

#define STAMP_DECL
#define STAMP(i)
#define STAMP_OUT(base, n)
__global__ __launch_bounds__(512, 2) void attn_kernel(const uint16_t* __restrict__ x, const uint16_t* __restrict__ kc,
                                                      const uint16_t* __restrict__ vc, const uint16_t* __restrict__ wq,
                                                      uint16_t* __restrict__ ofrag) {
  extern __shared__ __attribute__((aligned(16))) char smem[];
  const unsigned tid = threadIdx.x, lane = tid & 63;
  const unsigned wid = rfl(tid >> 6), g = wid >> 2, r = wid & 3;
  const unsigned l32 = lane & 31, hi = lane >> 5;
  const unsigned bid = blockIdx.x, xcd = bid & 7, idx = bid >> 3;
  const unsigned h = 4 * xcd + (idx & 3), b = idx >> 2;
  const unsigned lds0 = rfl((unsigned)(uintptr_t)smem);

  const unsigned l4 = lane >> 4;
  const size_t kvoff = (size_t)(b * NH + h) * SK * DH;
  const rsrc_t rsKV = mkrsrc((g ? vc : kc) + kvoff, SK * DH * 2);
  const rsrc_t rsQ = g ? mkrsrc(wq + h * DH, DM * NH * DH * 2 - h * DH * 2) : mkrsrc(x + (size_t)b * SQ * DM, SQ * DM * 2);
  const unsigned voffKV = (4 * r + l4) * 256 + 16 * ((lane & 15) ^ (g ? (l4 << 2) : (4 * r + l4)));
  const unsigned voffQ = g ? (4 * r + l4) * 8192 + 16 * ((lane & 15) ^ (l4 << 2))
                           : (8 * r + (lane >> 3)) * 2048 + 16 * ((lane & 7) ^ (4 * (r & 1) + l4));
  const unsigned qstS = g ? 64 * 8192 : 128, qstJ = g ? 16 * 8192 : 32 * 2048;
  const unsigned dstw = lds0 + g * 16384 + r * 1024;
  const unsigned kstart = (2 * b + 4 * (h & 3)) & 15;

  auto issueQ = [&](unsigned i, unsigned slot) {
    const unsigned so = ((i + kstart) & 15) * qstS;
#pragma unroll
    for (int j = 0; j < 4; ++j) dma<0>(dstw + slot * SLOT + j * 4096, voffQ, rsQ, so + j * qstJ);
  };
  auto issueKV = [&](unsigned t, unsigned slot) {
#pragma unroll
    for (int j = 0; j < 4; ++j) dma<1>(dstw + slot * SLOT + j * 4096, voffKV, rsKV, t * 16384 + j * 4096);
  };

  asm volatile("s_nop 4" ::: "memory");
  issueQ(0, 3); issueQ(1, 2); issueQ(2, 4);
  issueKV(0, 0); issueKV(1, 1);

  const unsigned q4 = (lane & 15) >> 2, p4 = lane & 3, blk = (lane >> 4) & 1;
  const unsigned xrow = 32 * r + l32;
  const unsigned baseX = lds0 + 128 * xrow + 16 * (hi ^ ((xrow >> 1) & 7));
  const unsigned baseW = lds0 + 16384 + 256 * (8 * hi + q4) + 16 * (4 * ((2 * g) ^ q4) + 2 * blk + (p4 & 1)) + 8 * (p4 >> 1);

  f32x16 qa0 = {}, qa1 = {};
  auto qstage = [&](unsigned slot) {
    const unsigned sx = baseX + slot * SLOT, sw0 = baseW + slot * SLOT, sw1 = sw0 ^ 64;
#pragma unroll
    for (int s = 0; s < 4; ++s) {
      const bf16x8 xb = lds_b128(sx ^ (s << 5));
      const bf16x8 w0 = cat(lds_tr(sw0 + 4096 * s), lds_tr(sw0 + 4096 * s + 1024));
      const bf16x8 w1 = cat(lds_tr(sw1 + 4096 * s), lds_tr(sw1 + 4096 * s + 1024));
      qa0 = MFMA(w0, xb, qa0);
      qa1 = MFMA(w1, xb, qa1);
    }
  };
  wait_bar<16>(); qstage(3);
  wait_bar<12>(); issueQ(3, 3); qstage(2);
  wait_bar<12>(); issueQ(4, 2); qstage(4);
  {
    unsigned scur = 3, snext = 2, sprev = 4;
    for (unsigned i = 3; i < 14; ++i) {
      wait_bar<4>();
      issueQ(i + 2, sprev);
      qstage(scur);
      const unsigned tmp = sprev; sprev = scur; scur = snext; snext = tmp;
    }
  }
  wait_bar<4>(); issueKV(2, 2); qstage(4);
  wait_bar<4>(); qstage(3);
  bf16x8 qf[8];
  {
    const unsigned xw = lds0 + 4 * SLOT + (r * 8 + 4 * g) * 1024 + lane * 16;
    *(LAS bf16x8*)(uintptr_t)(xw) = pack8<0>(qa0);
    *(LAS bf16x8*)(uintptr_t)(xw + 1024) = pack8<1>(qa0);
    *(LAS bf16x8*)(uintptr_t)(xw + 2048) = pack8<0>(qa1);
    *(LAS bf16x8*)(uintptr_t)(xw + 3072) = pack8<1>(qa1);
    wait_bar<63>();
    issueKV(3, 3);
    const unsigned xr = lds0 + 4 * SLOT + r * 8192 + lane * 16;
#pragma unroll
    for (int f = 0; f < 8; ++f) qf[f] = lds_b128(xr + f * 1024);
  }

  const unsigned baseK = lds0 + 8192 * g + 256 * l32 + 16 * (hi ^ (l32 & 15));
  const unsigned baseV = lds0 + 16384 + 8192 * g + 256 * (4 * hi + q4) + 16 * (4 * q4 + 2 * blk + (p4 >> 1)) + 8 * (p4 & 1);
  f32x16 o0 = {}, o1 = {}, o2 = {}, o3 = {};
  float m = -__builtin_inff(), l = 0.f;

  auto step = [&](unsigned slot) {
    const unsigned sk = baseK + slot * SLOT, sv = baseV + slot * SLOT;
    f32x16 s = {};
#pragma unroll
    for (int k = 0; k < 8; ++k) s = MFMA(lds_b128(sk ^ (k << 5)), qf[k], s);
    bf16x8 vf[4][2];
#pragma unroll
    for (int db = 0; db < 4; ++db)
#pragma unroll
      for (int ks = 0; ks < 2; ++ks) {
        const unsigned a = (sv ^ (db << 6)) + 4096 * ks;
        vf[db][ks] = cat(lds_tr(a), lds_tr(a + 2048));
      }
    float pm = fmaxf(fmaxf(s[0], s[1]), s[2]);
#pragma unroll
    for (int i = 3; i < 15; i += 2) pm = fmaxf(fmaxf(pm, s[i]), s[i + 1]);
    pm = fmaxf(pm, s[15]);
    pm = swap_max(pm);
    if (__any(pm > m)) {
      const float mn = fmaxf(m, pm);
      const float alpha = __builtin_amdgcn_exp2f((m - mn) * CEXP);
      m = mn;
      l *= alpha;
      o0 *= alpha; o1 *= alpha; o2 *= alpha; o3 *= alpha;
    }
    const float nm = -m * CEXP;
    float sum = 0.f;
#pragma unroll
    for (int i = 0; i < 16; ++i) { s[i] = __builtin_amdgcn_exp2f(__builtin_fmaf(s[i], CEXP, nm)); sum += s[i]; }
    l += sum;
    const bf16x8 p0 = pack8<0>(s), p1 = pack8<1>(s);
    o0 = MFMA(vf[0][0], p0, o0); o1 = MFMA(vf[1][0], p0, o1); o2 = MFMA(vf[2][0], p0, o2); o3 = MFMA(vf[3][0], p0, o3);
    o0 = MFMA(vf[0][1], p1, o0); o1 = MFMA(vf[1][1], p1, o1); o2 = MFMA(vf[2][1], p1, o2); o3 = MFMA(vf[3][1], p1, o3);
  };
  {
    unsigned slot = 0, fill = 4;
    for (unsigned t = 0; t < NT - 4; ++t) {
      wait_bar<12>();
      issueKV(t + 4, fill);
      step(slot);
      fill = slot;
      slot = (slot == 4) ? 0 : slot + 1;
    }
  }
  wait_bar<12>(); step((NT - 4) % 5);
  wait_bar<8>();  step((NT - 3) % 5);
  wait_bar<4>();  step((NT - 2) % 5);
  wait_bar<0>();  step((NT - 1) % 5);

  l = swap_sum(l);
  {
    const unsigned xw = lds0 + 2 * SLOT + wid * 8192 + lane * 16;
    const f32x16 sa = g ? o0 : o2, sb = g ? o1 : o3;
#pragma unroll
    for (int i = 0; i < 4; ++i) {
      *(LAS f32x4*)(uintptr_t)(xw + i * 1024) = (f32x4){sa[4 * i], sa[4 * i + 1], sa[4 * i + 2], sa[4 * i + 3]};
      *(LAS f32x4*)(uintptr_t)(xw + 4096 + i * 1024) = (f32x4){sb[4 * i], sb[4 * i + 1], sb[4 * i + 2], sb[4 * i + 3]};
    }
    *(LAS f32x2*)(uintptr_t)(lds0 + 4 * SLOT + wid * 512 + lane * 8) = (f32x2){m, l};
    wait_bar<0>();
    const unsigned pw = wid ^ 4;
    const f32x2 ml = *(const LAS f32x2*)(uintptr_t)(lds0 + 4 * SLOT + pw * 512 + lane * 8);
    const float mf = fmaxf(m, ml[0]);
    const float a0 = __builtin_amdgcn_exp2f((m - mf) * CEXP), a1 = __builtin_amdgcn_exp2f((ml[0] - mf) * CEXP);
    const float inv = 1.0f / (a0 * l + a1 * ml[1]);
    const float c0 = a0 * inv, c1 = a1 * inv;
    f32x16 ea = g ? o2 : o0, eb = g ? o3 : o1;
    const unsigned xr = lds0 + 2 * SLOT + pw * 8192 + lane * 16;
#pragma unroll
    for (int i = 0; i < 4; ++i) {
      const f32x4 ua = *(const LAS f32x4*)(uintptr_t)(xr + i * 1024);
      const f32x4 ub = *(const LAS f32x4*)(uintptr_t)(xr + 4096 + i * 1024);
#pragma unroll
      for (int e = 0; e < 4; ++e) {
        ea[4 * i + e] = ea[4 * i + e] * c0 + ua[e] * c1;
        eb[4 * i + e] = eb[4 * i + e] * c0 + ub[e] * c1;
      }
    }
    uint16_t* op = ofrag + ((size_t)(((b * NH + h) * 4 + r) * 8 + 4 * g) * 512 + lane * 8);
    *(bf16x8*)(op) = pack8<0>(ea);
    *(bf16x8*)(op + 512) = pack8<1>(ea);
    *(bf16x8*)(op + 1024) = pack8<0>(eb);
    *(bf16x8*)(op + 1536) = pack8<1>(eb);
  }
}

__global__ __launch_bounds__(512, 2) void oproj_kernel(const uint16_t* __restrict__ ofrag, const uint16_t* __restrict__ wo,
                                                       uint16_t* __restrict__ out) {
  extern __shared__ __attribute__((aligned(16))) char smem[];
  const unsigned tid = threadIdx.x, lane = tid & 63;
  const unsigned wid = rfl(tid >> 6);
  const unsigned l32 = lane & 31, hi = lane >> 5;
  const unsigned bid = blockIdx.x, xcd = bid & 7, idx = bid >> 3;
  const unsigned tm = 4 * (xcd & 3) + (idx & 3), tn = 8 * (xcd >> 2) + (idx >> 2);
  const unsigned b = tm >> 1, qb0 = 2 * (tm & 1);
  const unsigned lds0 = rfl((unsigned)(uintptr_t)smem);
  const unsigned ring = lds0 + wid * 20480;

  const rsrc_t rsW = mkrsrc(wo + (size_t)wid * 512 * DM + tn * 64, 512 * DM * 2 - tn * 128);
  const rsrc_t rsO = mkrsrc(ofrag + (size_t)(b * NH + 4 * wid) * 4 * 8 * 512, 4 * 4 * 8 * 1024);
  const unsigned rw = lane >> 3;
  const unsigned voffW = rw * 2048 + 16 * ((lane & 7) ^ (((rw >> 1) & 1) << 2));
  const unsigned voffO = lane * 16 + qb0 * 8192;
  auto issue = [&](unsigned c, unsigned slot) {
    const unsigned d = ring + slot * 4096;
    dma<0>(d, voffW, rsW, c * 16 * 2048);
    dma<0>(d + 1024, voffW, rsW, c * 16 * 2048 + 8 * 2048);
    const unsigned so = (c >> 3) * 32768 + (c & 7) * 1024;
    dma<0>(d + 2048, voffO, rsO, so);
    dma<0>(d + 3072, voffO, rsO, so + 8192);
  };
  asm volatile("s_nop 4" ::: "memory");
  issue(0, 0); issue(1, 1); issue(2, 2); issue(3, 3);

  const unsigned q4 = (lane & 15) >> 2, p4 = lane & 3, blk = (lane >> 4) & 1;
  const unsigned baseA = ring + 128 * (4 * hi + q4) + 16 * (((q4 >> 1) << 2) + 2 * blk + (p4 >> 1)) + 8 * (p4 & 1);
  const unsigned baseB = ring + 2048 + lane * 16;
  f32x16 a00 = {}, a01 = {}, a10 = {}, a11 = {};
  auto comp = [&](unsigned slot) {
    const unsigned sa = baseA + slot * 4096, sb = baseB + slot * 4096;
    const bf16x8 w0 = cat(lds_tr(sa), lds_tr(sa + 1024));
    const bf16x8 w1 = cat(lds_tr(sa ^ 64), lds_tr((sa ^ 64) + 1024));
    const bf16x8 b0 = lds_b128(sb), b1 = lds_b128(sb + 1024);
    a00 = MFMA(w0, b0, a00); a01 = MFMA(w0, b1, a01);
    a10 = MFMA(w1, b0, a10); a11 = MFMA(w1, b1, a11);
  };
  {
    unsigned slot = 0, fill = 4;
    for (unsigned c = 0; c < 28; ++c) {
      wait_vm<12>();
      issue(c + 4, fill);
      comp(slot);
      fill = slot;
      slot = (slot == 4) ? 0 : slot + 1;
    }
  }
  wait_vm<12>(); comp(28 % 5);
  wait_vm<8>();  comp(29 % 5);
  wait_vm<4>();  comp(30 % 5);
  wait_vm<0>();  comp(31 % 5);
  wait_vm<0>();
  {
    const unsigned xw = ring + lane * 16;
#pragma unroll
    for (int i = 0; i < 4; ++i) {
      *(LAS f32x4*)(uintptr_t)(xw + i * 1024) = (f32x4){a00[4 * i], a00[4 * i + 1], a00[4 * i + 2], a00[4 * i + 3]};
      *(LAS f32x4*)(uintptr_t)(xw + 4096 + i * 1024) = (f32x4){a01[4 * i], a01[4 * i + 1], a01[4 * i + 2], a01[4 * i + 3]};
      *(LAS f32x4*)(uintptr_t)(xw + 8192 + i * 1024) = (f32x4){a10[4 * i], a10[4 * i + 1], a10[4 * i + 2], a10[4 * i + 3]};
      *(LAS f32x4*)(uintptr_t)(xw + 12288 + i * 1024) = (f32x4){a11[4 * i], a11[4 * i + 1], a11[4 * i + 2], a11[4 * i + 3]};
    }
    wait_bar<0>();
    const unsigned xr = lds0 + wid * 2048 + lane * 16;
    f32x4 u0 = {}, u1 = {};
#pragma unroll
    for (int w = 0; w < 8; ++w) {
      u0 += *(const LAS f32x4*)(uintptr_t)(xr + w * 20480);
      u1 += *(const LAS f32x4*)(uintptr_t)(xr + w * 20480 + 1024);
    }
    const unsigned nb = wid >> 2, qb = (wid >> 1) & 1;
    unsigned x0 = cvtpk(u0[0], u0[1]), x1 = cvtpk(u0[2], u0[3]), y0 = cvtpk(u1[0], u1[1]), y1 = cvtpk(u1[2], u1[3]);
    auto r0 = __builtin_amdgcn_permlane32_swap(x0, y0, false, false);
    auto r1 = __builtin_amdgcn_permlane32_swap(x1, y1, false, false);
    const u32x4 w4 = {r0[0], r1[0], r0[1], r1[1]};
    uint16_t* op = out + (size_t)(tm * 64 + qb * 32 + l32) * DM + tn * 64 + nb * 32 + 16 * (wid & 1) + 8 * hi;
    *(u32x4*)op = w4;
  }
}

extern "C" void kernel_launch(void* const* d_in, const int* in_sizes, int n_in, void* d_out, int out_size, void* d_ws,
                              size_t ws_size, hipStream_t stream) {
  const uint16_t* x = (const uint16_t*)d_in[0];
  const uint16_t* kc = (const uint16_t*)d_in[1];
  const uint16_t* vc = (const uint16_t*)d_in[2];
  const uint16_t* wq = (const uint16_t*)d_in[3];
  const uint16_t* wo = (const uint16_t*)d_in[4];
  uint16_t* ofrag = (uint16_t*)d_ws;
  (void)hipFuncSetAttribute((const void*)attn_kernel, hipFuncAttributeMaxDynamicSharedMemorySize, LDS1);
  (void)hipFuncSetAttribute((const void*)oproj_kernel, hipFuncAttributeMaxDynamicSharedMemorySize, LDS2);
  hipLaunchKernelGGL(attn_kernel, dim3(256), dim3(512), LDS1, stream, x, kc, vc, wq, ofrag);
  hipLaunchKernelGGL(oproj_kernel, dim3(256), dim3(512), LDS2, stream, (const uint16_t*)ofrag, wo, (uint16_t*)d_out);
}
```

```cpp
#include <hip/hip_runtime.h>
#include <hip/hip_bf16.h>
#include <stdint.h>

typedef float f32x16 __attribute__((ext_vector_type(16)));
typedef float f32x4 __attribute__((ext_vector_type(4)));
typedef float f32x2 __attribute__((ext_vector_type(2)));
typedef __bf16 bf16x8 __attribute__((ext_vector_type(8)));
typedef __bf16 bf16x2 __attribute__((ext_vector_type(2)));
typedef short s16x4 __attribute__((ext_vector_type(4)));
typedef short s16x8 __attribute__((ext_vector_type(8)));
typedef unsigned u32x4 __attribute__((ext_vector_type(4)));
typedef unsigned u32x2 __attribute__((ext_vector_type(2)));
typedef __amdgpu_buffer_rsrc_t rsrc_t;

#define LAS __attribute__((address_space(3)))
#define DI __device__ __forceinline__
#define MFMA(a, b, c) __builtin_amdgcn_mfma_f32_32x32x16_bf16((a), (b), (c), 0, 0, 0)
#define SBAR() __builtin_amdgcn_sched_barrier(0)

constexpr int NB = 8, SQ = 128, DM = 1024, NH = 32, SK = 2048, DH = 128;
constexpr int NT = SK / 64;
constexpr unsigned SLOT = 32768;
constexpr int LDS1 = 5 * 32768;
constexpr int LDS2 = 5 * 32768;
constexpr float CEXP = 0.12751743082459868f;
#define THR 8.0f
constexpr float THR_RAW = THR / CEXP;
#define DIST 2
#define PRIO 0
#define XPOL 0
#define WPOL 0

DI unsigned rfl(unsigned x) { return __builtin_amdgcn_readfirstlane(x); }
DI rsrc_t mkrsrc(const void* p, unsigned bytes) {
  return __builtin_amdgcn_make_buffer_rsrc((void*)p, (short)0, (int)bytes, 0x00020000);
}
template <int NTF>
DI void dma(unsigned dst, unsigned voff, rsrc_t rs, unsigned soff) {
  if constexpr (NTF)
    asm volatile("s_mov_b32 m0, %0\n\ts_nop 0\n\tbuffer_load_dwordx4 %1, %2, %3 offen nt lds" ::"s"(dst), "v"(voff), "s"(rs), "s"(soff) : "memory");
  else
    asm volatile("s_mov_b32 m0, %0\n\ts_nop 0\n\tbuffer_load_dwordx4 %1, %2, %3 offen lds" ::"s"(dst), "v"(voff), "s"(rs), "s"(soff) : "memory");
}
template <int N>
DI void wait_bar() { asm volatile("s_waitcnt vmcnt(%0) lgkmcnt(0)\n\ts_barrier" ::"i"(N) : "memory"); }
template <int N>
DI void wait_vm() { asm volatile("s_waitcnt vmcnt(%0) lgkmcnt(0)" ::"i"(N) : "memory"); }

DI bf16x8 lds_b128(unsigned addr) { return *(const LAS bf16x8*)(uintptr_t)addr; }
DI s16x4 lds_tr(unsigned addr) { return __builtin_amdgcn_ds_read_tr16_b64_v4i16((LAS s16x4*)(uintptr_t)addr); }
DI bf16x8 cat(s16x4 a, s16x4 b) { return __builtin_bit_cast(bf16x8, __builtin_shufflevector(a, b, 0, 1, 2, 3, 4, 5, 6, 7)); }
DI unsigned cvtpk(float lo, float hi) {
  f32x2 v = {lo, hi};
  return __builtin_bit_cast(unsigned, __builtin_convertvector(v, bf16x2));
}
template <int S>
DI bf16x8 pack8(const f32x16& v) {
  u32x4 w = {cvtpk(v[8 * S + 0], v[8 * S + 1]), cvtpk(v[8 * S + 2], v[8 * S + 3]), cvtpk(v[8 * S + 4], v[8 * S + 5]), cvtpk(v[8 * S + 6], v[8 * S + 7])};
  return __builtin_bit_cast(bf16x8, w);
}
DI float swap_max(float v) {
  auto rr = __builtin_amdgcn_permlane32_swap(__float_as_uint(v), __float_as_uint(v), false, false);
  return fmaxf(__uint_as_float(rr[0]), __uint_as_float(rr[1]));
}
DI float swap_sum(float v) {
  auto rr = __builtin_amdgcn_permlane32_swap(__float_as_uint(v), __float_as_uint(v), false, false);
  return __uint_as_float(rr[0]) + __uint_as_float(rr[1]);
}

#define STAMP_DECL
#define STAMP(i)
#define STAMP_OUT(base, n)
#define ABL_HNOMM 0
#define ABL_HNORD 0
#define ABL_HNODMA 0
#define CODEPF 0
#define CODEBYTES 20480
#define PIN(x) asm volatile("" : "+v"(x))
#define FOLD 1
#define PRIOAB 1
#define TMASK_A 0x11u
#define TMASK_B 0x44u
#define ISSK 0xFFFFFFFFu
#define ISSV 0xFFFFFFFCu
#define ABL_NODMA 0
struct Sched { unsigned iss, w3, w2, w1; };
constexpr Sched mksched(unsigned want, int G) {
  Sched s{0, 0, 0, 0};
  int nxt = 5;
  for (int st = 0; st <= NT - 2; ++st) {
    const int lim = G ? st + 3 : st + 5;
    const int need = G ? st : st + 2;
    const int lastneed = G ? (st == NT - 2 ? NT - 1 : st) : st + 2;
    if (nxt < NT && nxt <= lim && (((want >> st) & 1) || nxt <= lastneed)) { s.iss |= 1u << st; ++nxt; }
    int y = nxt - need - 1;
    if (need >= NT || (G && st == NT - 2)) y = 3;
    if (y >= 3) s.w3 |= 1u << st; else if (y == 2) s.w2 |= 1u << st; else if (y == 1) s.w1 |= 1u << st;
  }
  return s;
}
constexpr int schedcount(unsigned m) { int c = 0; for (int i = 0; i < 32; ++i) c += (m >> i) & 1; return c; }
static_assert(schedcount(mksched(ISSK, 0).iss) == NT - 5 && schedcount(mksched(ISSV, 1).iss) == NT - 5, "schedule must issue every tile");

template <int NTF>
DI void dma4(unsigned dst, unsigned va, unsigned vb, unsigned vc_, unsigned vd, rsrc_t rs, unsigned s0, unsigned s1, unsigned s2, unsigned s3) {
  if constexpr (NTF)
    asm volatile("s_mov_b32 m0, %0\n\ts_nop 0\n\t"
                 "buffer_load_dwordx4 %1, %5, %6 offen nt lds\n\t"
                 "buffer_load_dwordx4 %2, %5, %7 offen offset:1024 nt lds\n\t"
                 "buffer_load_dwordx4 %3, %5, %8 offen offset:2048 nt lds\n\t"
                 "buffer_load_dwordx4 %4, %5, %9 offen offset:3072 nt lds"
                 ::"s"(dst), "v"(va), "v"(vb), "v"(vc_), "v"(vd), "s"(rs), "s"(s0), "s"(s1), "s"(s2), "s"(s3) : "memory");
  else
    asm volatile("s_mov_b32 m0, %0\n\ts_nop 0\n\t"
                 "buffer_load_dwordx4 %1, %5, %6 offen lds\n\t"
                 "buffer_load_dwordx4 %2, %5, %7 offen offset:1024 lds\n\t"
                 "buffer_load_dwordx4 %3, %5, %8 offen offset:2048 lds\n\t"
                 "buffer_load_dwordx4 %4, %5, %9 offen offset:3072 lds"
                 ::"s"(dst), "v"(va), "v"(vb), "v"(vc_), "v"(vd), "s"(rs), "s"(s0), "s"(s1), "s"(s2), "s"(s3) : "memory");
}
DI void dma4_if(unsigned mask, unsigned st, unsigned dst, unsigned va, unsigned vb, rsrc_t rs, unsigned so) {
  asm volatile("s_bitcmp1_b32 %0, %1\n\ts_cbranch_scc0 1f\n\t"
               "s_mov_b32 m0, %2\n\ts_nop 0\n\t"
               "buffer_load_dwordx4 %3, %5, %6 offen nt lds\n\t"
               "buffer_load_dwordx4 %3, %5, %6 offen offset:1024 nt lds\n\t"
               "buffer_load_dwordx4 %4, %5, %6 offen offset:2048 nt lds\n\t"
               "buffer_load_dwordx4 %4, %5, %6 offen offset:3072 nt lds\n"
               "1:"
               ::"s"(mask), "s"(st), "s"(dst), "v"(va), "v"(vb), "s"(rs), "s"(so) : "memory", "scc");
}
DI void wait_sched_bar(unsigned w3, unsigned w2, unsigned w1, unsigned st) {
  asm volatile("s_waitcnt vmcnt(12)\n\t"
               "s_bitcmp1_b32 %0, %3\n\ts_cbranch_scc1 1f\n\t"
               "s_waitcnt vmcnt(8)\n\t"
               "s_bitcmp1_b32 %1, %3\n\ts_cbranch_scc1 1f\n\t"
               "s_waitcnt vmcnt(4)\n\t"
               "s_bitcmp1_b32 %2, %3\n\ts_cbranch_scc1 1f\n\t"
               "s_waitcnt vmcnt(0)\n"
               "1:\n\ts_waitcnt lgkmcnt(0)\n\ts_barrier"
               ::"s"(w3), "s"(w2), "s"(w1), "s"(st) : "memory", "scc");
}
DI void lbar() { asm volatile("s_waitcnt lgkmcnt(0)\n\ts_barrier" ::: "memory"); }
struct QFrag { bf16x8 xb[4]; s16x4 wa[4][2], wb[4][2]; };

#define CT(x)
#define CH(x)

#define IT(i)
template <int G>
DI void attn_body(const uint16_t* __restrict__ x, const uint16_t* __restrict__ kc, const uint16_t* __restrict__ vc,
                  const uint16_t* __restrict__ wq, uint16_t* __restrict__ ofrag, unsigned lds0, unsigned r, unsigned long long pc) {
  constexpr unsigned g = G;
  const unsigned lane = threadIdx.x & 63, wid = 4 * g + r;
  const unsigned l32 = lane & 31, hi = lane >> 5;
  const unsigned bid = blockIdx.x, xcd = bid & 7, idx = bid >> 3;
  const unsigned h = 4 * xcd + (idx & 3), b = idx >> 2;
  STAMP_DECL
  unsigned itr_ = 0, itc_ = 0;
  STAMP(0);
  IT(0);

  const unsigned l4 = lane >> 4;
  const size_t kvoff = (size_t)(b * NH + h) * SK * DH;
  const rsrc_t rsKV = mkrsrc((g ? vc : kc) + kvoff, SK * DH * 2);
  const rsrc_t rsQ = g ? mkrsrc(wq + h * DH, DM * NH * DH * 2 - h * DH * 2) : mkrsrc(x + (size_t)b * SQ * DM, SQ * DM * 2);
  const unsigned voffA = (16 * r + l4) * 256 + 16 * ((lane & 15) ^ (g ? (l4 << 2) : (l4 | ((r & 1) << 3))));
  const unsigned voffB = g ? voffA : voffA ^ 64;
  const unsigned voffQ = g ? (16 * r + l4) * 8192 + 16 * ((lane & 15) ^ (l4 << 2))
                           : (32 * r + (lane >> 3)) * 2048 + 16 * ((lane & 7) ^ l4);
  const unsigned voffQ1 = g ? voffQ : voffQ ^ 64;
  constexpr unsigned qstS = g ? 64 * 8192 : 128, qstJ = (g ? 4 * 8192 : 8 * 2048) - 1024;
  const unsigned dstw = lds0 + g * 16384 + r * 4096;
  const unsigned kstart = (2 * b + 4 * (h & 3)) & 15;

  auto issueQ = [&](unsigned i, unsigned so_) {
    if (ABL_HNODMA && i > 2) return;
    const unsigned so = ((i + kstart) & 15) * qstS;
    dma4<g ? WPOL : XPOL>(dstw + so_, voffQ, voffQ1, voffQ, voffQ1, rsQ, so, so + qstJ, so + 2 * qstJ, so + 3 * qstJ);
  };
  auto issueKV = [&](unsigned t, unsigned so_) {
    const unsigned so = t * 16384;
    dma4<1>(dstw + so_, voffA, voffA, voffB, voffB, rsKV, so, so, so, so);
  };

  asm volatile("s_nop 4" ::: "memory");
  issueQ(0, 4 * SLOT); issueQ(1, 2 * SLOT);

  const unsigned q4 = (lane & 15) >> 2, p4 = lane & 3, blk = (lane >> 4) & 1;
  const unsigned xrow = 32 * r + l32;
  const unsigned baseX = lds0 + 128 * xrow + 16 * (hi ^ ((xrow >> 1) & 7));
  const unsigned baseW = lds0 + 16384 + 256 * (8 * hi + q4) + 16 * (4 * ((2 * g) ^ q4) + 2 * blk + (p4 & 1)) + 8 * (p4 >> 1);

  f32x16 qa0 = {}, qa1 = {};
  QFrag fa, fb;
  auto qiter = [&](QFrag& cur, QFrag& nxt, unsigned so_, bool rd, bool mm) {
    const unsigned sx = baseX + so_, sw0 = baseW + so_, sw1 = sw0 ^ 64;
#pragma unroll
    for (int s = 0; s < 4; ++s) {
      if (rd && !(ABL_HNORD && mm)) { nxt.xb[s] = lds_b128(sx ^ (s << 5)); nxt.wa[s][0] = lds_tr(sw0 + 4096 * s); nxt.wa[s][1] = lds_tr(sw0 + 4096 * s + 1024); }
      SBAR();
      if (mm && !ABL_HNOMM) qa0 = MFMA(cat(cur.wa[s][0], cur.wa[s][1]), cur.xb[s], qa0);
      SBAR();
      if (rd && !(ABL_HNORD && mm)) { nxt.wb[s][0] = lds_tr(sw1 + 4096 * s); nxt.wb[s][1] = lds_tr(sw1 + 4096 * s + 1024); }
      SBAR();
      if (mm && !ABL_HNOMM) qa1 = MFMA(cat(cur.wb[s][0], cur.wb[s][1]), cur.xb[s], qa1);
      SBAR();
      if (ABL_HNOMM && rd) { PIN(nxt.xb[s]); PIN(nxt.wa[s][0]); PIN(nxt.wa[s][1]); PIN(nxt.wb[s][0]); PIN(nxt.wb[s][1]); }
    }
  };
  STAMP(1);
  const unsigned tmask = (idx & 1) ? TMASK_B : TMASK_A;
  unsigned tnext = 0;
  auto tissue = [&](unsigned bit) {
    dma4_if(tmask, bit, dstw + tnext * SLOT, voffA, voffB, rsKV, tnext * 16384);
    tnext += (tmask >> bit) & 1;
  };
  wait_bar<4>();
  STAMP(2);
  IT(1);
  qiter(fb, fa, 4 * SLOT, true, false);
  if (ABL_HNORD) qiter(fa, fb, 4 * SLOT, true, false);
  issueQ(2, 3 * SLOT); tissue(0);
  {
    unsigned scur = 4 * SLOT, snext = 2 * SLOT;
    auto hiter = [&](unsigned i, QFrag& cur, QFrag& nxt, int kv) {
      unsigned long long t0_, t2_, t3_, t4_, t5_;
      CH(t0_);
      wait_bar<4>();
      IT(i + 2);
      CH(t2_);
      if (g) { if (kv) issueKV(kv, scur); else { issueQ(i + 3, scur); tissue(i + 1); } }
      CH(t3_);
      qiter(cur, nxt, snext, true, true);
      CH(t4_);
      if (!g) { if (kv) issueKV(kv, scur); else { issueQ(i + 3, scur); tissue(i + 1); } }
      CH(t5_);
      scur = snext; snext = (snext == 4 * SLOT) ? 2 * SLOT : snext + SLOT;
    };
    for (unsigned i = 0; i < 12; i += 2) { hiter(i, fa, fb, 0); hiter(i + 1, fb, fa, 0); }
    STAMP(3);
    hiter(12, fa, fb, 0);
    hiter(13, fb, fa, 2);
    STAMP(4);
    hiter(14, fa, fb, 3);
  }
  qiter(fb, fa, 0, false, true);
  bf16x8 qf[8];
  {
    if (FOLD) { qa0 *= CEXP; qa1 *= CEXP; }
    const bf16x8 e0 = pack8<0>(qa0), e1 = pack8<1>(qa0), e2 = pack8<0>(qa1), e3 = pack8<1>(qa1);
    lbar();
    const unsigned xw = lds0 + 4 * SLOT + wid * 4096 + lane * 16;
    *(LAS bf16x8*)(uintptr_t)(xw) = e0;
    *(LAS bf16x8*)(uintptr_t)(xw + 1024) = e1;
    *(LAS bf16x8*)(uintptr_t)(xw + 2048) = e2;
    *(LAS bf16x8*)(uintptr_t)(xw + 3072) = e3;
    lbar();
    const unsigned xr = lds0 + 4 * SLOT + (wid ^ 4) * 4096 + lane * 16;
    qf[4 * g + 0] = e0; qf[4 * g + 1] = e1; qf[4 * g + 2] = e2; qf[4 * g + 3] = e3;
#pragma unroll
    for (int f = 0; f < 4; ++f) qf[4 * (1 - g) + f] = lds_b128(xr + f * 1024);
  }

  const unsigned baseK = lds0 + 8192 * g + 256 * l32 + 16 * (hi ^ ((l32 & 3) | ((l32 >> 3) << 2)));
  const unsigned baseV = lds0 + 16384 + 8192 * g + 256 * (4 * hi + q4) + 16 * (4 * q4 + 2 * blk + (p4 >> 1)) + 8 * (p4 & 1);
  f32x16 o0 = {}, o1 = {}, o2 = {}, o3 = {};
  f32x16 sA, sB, negm;
  bf16x8 kf[8];
  s16x4 va[8], vb[8];
  u32x4 pw0, pw1;
  float m, l = 0.f, alpha = 1.f;
  bool resc = false;

  auto rowmax = [&](const f32x16& s) {
    float a = fmaxf(fmaxf(s[0], s[1]), s[2]), c = fmaxf(fmaxf(s[3], s[4]), s[5]);
    a = fmaxf(fmaxf(a, s[6]), s[7]); c = fmaxf(fmaxf(c, s[8]), s[9]);
    a = fmaxf(fmaxf(a, s[10]), s[11]); c = fmaxf(fmaxf(c, s[12]), s[13]);
    a = fmaxf(fmaxf(a, s[14]), s[15]);
    return swap_max(fmaxf(a, c));
  };
#define EX(v) __builtin_amdgcn_exp2f(v)
  auto decide = [&](f32x16& s) {
    const float rm = FOLD ? rowmax(s) : __builtin_fmaf(rowmax(s), CEXP, -m);
    resc = false;
    if (__builtin_expect(__any(rm > THR), 0)) {
      const float dl = fmaxf(rm, 0.f);
      m += dl;
      alpha = __builtin_amdgcn_exp2f(-dl);
      l *= alpha;
      if (FOLD) { s -= dl; negm -= dl; }
      resc = true;
    }
  };
#define VRD(i, sv) do { const unsigned a_ = ((sv) ^ (((i) & 3) << 6)) + 4096 * ((i) >> 2); va[i] = lds_tr(a_); vb[i] = lds_tr(a_ + 2048); } while (0)
#define PB(k) __builtin_bit_cast(bf16x8, pw##k)
  auto phaseA = [&](f32x16& sc, f32x16& pp, unsigned sv, bool vr) {
    float sacc = 0.f;
    if (PRIOAB) __builtin_amdgcn_s_setprio(1);
#pragma unroll
    for (int k = 0; k < 8; ++k) {
      if (vr) VRD(k, sv);
      SBAR();
      sc = MFMA(kf[k], qf[k], k == 0 ? negm : sc);
      if (k == 0) sacc = pp[0] + pp[1]; else { sacc += pp[2 * k]; sacc += pp[2 * k + 1]; }
      if (k < 4) { pw0[k] = cvtpk(pp[2 * k], pp[2 * k + 1]); PIN(pw0); } else { pw1[k - 4] = cvtpk(pp[2 * k], pp[2 * k + 1]); PIN(pw1); }
      PIN(sacc);
      SBAR();
    }
    l += sacc;
    asm volatile("" ::: "memory");
  };
  auto phaseB = [&](f32x16& sc, unsigned sk) {
    const float nm = -m;
    if (PRIOAB) __builtin_amdgcn_s_setprio(0);
    SBAR();
#pragma unroll
    for (int i = 0; i < 8; ++i) {
      if (i < 4) { kf[2 * i] = lds_b128(sk ^ (i << 6)); kf[2 * i + 1] = lds_b128(sk ^ ((2 * i + 1) << 5)); }
      SBAR();
      const bf16x8 vf = cat(va[i], vb[i]);
      const bf16x8 pf = i < 4 ? PB(0) : PB(1);
      if ((i & 3) == 0) o0 = MFMA(vf, pf, o0);
      if ((i & 3) == 1) o1 = MFMA(vf, pf, o1);
      if ((i & 3) == 2) o2 = MFMA(vf, pf, o2);
      if ((i & 3) == 3) o3 = MFMA(vf, pf, o3);
      sc[2 * i] = EX(sc[2 * i]); sc[2 * i + 1] = EX(sc[2 * i + 1]);
      PIN(sc);
      SBAR();
    }
  };
  auto rescale = [&]() { if (__builtin_expect(resc, 0)) { o0 *= alpha; o1 *= alpha; o2 *= alpha; o3 *= alpha; } };

  STAMP(5);
  IT(17);
  constexpr Sched SC = mksched(g ? ISSV : ISSK, g);
  unsigned nso = 5 * 16384, fill = dstw;
  auto kvissue = [&](unsigned st) {
    if (ABL_NODMA) return;
    dma4_if(SC.iss, st, fill, voffA, voffB, rsKV, nso);
    if ((SC.iss >> st) & 1) { nso += 16384; fill = (fill == dstw + 4 * SLOT) ? dstw : fill + SLOT; }
  };
  {
#pragma unroll
    for (int k = 0; k < 8; ++k) kf[k] = lds_b128(baseK ^ (k << 5));
    lbar();
    issueKV(4, 4 * SLOT);
    sA = (f32x16){};
#pragma unroll
    for (int k = 0; k < 8; ++k) sA = MFMA(kf[k], qf[k], sA);
    SBAR();
#pragma unroll
    for (int k = 0; k < 8; ++k) kf[k] = lds_b128((baseK + SLOT) ^ (k << 5));
    m = FOLD ? rowmax(sA) : rowmax(sA) * CEXP;
    const float nm = -m;
#pragma unroll
    for (int i = 0; i < 16; ++i) { negm[i] = FOLD ? nm : 0.f; if (FOLD) sA[i] += nm; sA[i] = EX(sA[i]); }
    PIN(negm);
    if (!g) kvissue(0);
    wait_sched_bar(SC.w3, SC.w2, SC.w1, 0);
    IT(18);
  }
  {
    unsigned sok = 2 * SLOT, sov = 0;
    auto step = [&](unsigned j, f32x16& sc, f32x16& pp) {
      unsigned long long ta_, tb_, tc_, td_, te_;
      CT(ta_);
      if (g) kvissue(j);
      CT(tb_);
      phaseA(sc, pp, baseV + sov, true); decide(sc); phaseB(sc, baseK + sok);
      CT(tc_);
      if (!g) kvissue(j);
      sok = (sok == 4 * SLOT) ? 0 : sok + SLOT;
      sov = (sov == 4 * SLOT) ? 0 : sov + SLOT;
      CT(td_);
      wait_sched_bar(SC.w3, SC.w2, SC.w1, j);
      CT(te_);
      IT(18 + j);
      rescale();
    };
    for (unsigned j = 1; j < NT - 1; j += 2) { step(j, sB, sA); step(j + 1, sA, sB); }
  }
  STAMP(6);
  {
    phaseA(sB, sA, 0, false);
    decide(sB);
    const float nm = -m;
    float sacc = 0.f;
#pragma unroll
    for (int i = 0; i < 16; ++i) { sB[i] = EX(sB[i]); sacc += sB[i]; }
    l += sacc;
    const bf16x8 p0 = pack8<0>(sB), p1 = pack8<1>(sB);
    wait_bar<0>();
    STAMP(7);
    const unsigned sv = baseV + (NT - 2) % 5 * SLOT, su = baseV + (NT - 1) % 5 * SLOT;
#pragma unroll
    for (int i = 0; i < 8; ++i) VRD(i, sv);
    s16x4 ua[8], ub[8];
#pragma unroll
    for (int i = 0; i < 8; ++i) { const unsigned a_ = (su ^ ((i & 3) << 6)) + 4096 * (i >> 2); ua[i] = lds_tr(a_); ub[i] = lds_tr(a_ + 2048); }
    o0 = MFMA(cat(va[0], vb[0]), PB(0), o0); o1 = MFMA(cat(va[1], vb[1]), PB(0), o1);
    o2 = MFMA(cat(va[2], vb[2]), PB(0), o2); o3 = MFMA(cat(va[3], vb[3]), PB(0), o3);
    o0 = MFMA(cat(va[4], vb[4]), PB(1), o0); o1 = MFMA(cat(va[5], vb[5]), PB(1), o1);
    o2 = MFMA(cat(va[6], vb[6]), PB(1), o2); o3 = MFMA(cat(va[7], vb[7]), PB(1), o3);
    rescale();
    o0 = MFMA(cat(ua[0], ub[0]), p0, o0); o1 = MFMA(cat(ua[1], ub[1]), p0, o1);
    o2 = MFMA(cat(ua[2], ub[2]), p0, o2); o3 = MFMA(cat(ua[3], ub[3]), p0, o3);
    o0 = MFMA(cat(ua[4], ub[4]), p1, o0); o1 = MFMA(cat(ua[5], ub[5]), p1, o1);
    o2 = MFMA(cat(ua[6], ub[6]), p1, o2); o3 = MFMA(cat(ua[7], ub[7]), p1, o3);
  }
  STAMP(8);

  l = swap_sum(l);
  {
    const unsigned xw = lds0 + 2 * SLOT + wid * 8192 + lane * 16;
    const f32x16 sa = g ? o0 : o2, sb = g ? o1 : o3;
#pragma unroll
    for (int i = 0; i < 4; ++i) {
      *(LAS f32x4*)(uintptr_t)(xw + i * 1024) = (f32x4){sa[4 * i], sa[4 * i + 1], sa[4 * i + 2], sa[4 * i + 3]};
      *(LAS f32x4*)(uintptr_t)(xw + 4096 + i * 1024) = (f32x4){sb[4 * i], sb[4 * i + 1], sb[4 * i + 2], sb[4 * i + 3]};
    }
    *(LAS f32x2*)(uintptr_t)(lds0 + 4 * SLOT + wid * 512 + lane * 8) = (f32x2){m, l};
    lbar();
    const unsigned pw = wid ^ 4;
    const f32x2 ml = *(const LAS f32x2*)(uintptr_t)(lds0 + 4 * SLOT + pw * 512 + lane * 8);
    const float mf = fmaxf(m, ml[0]);
    const float a0 = __builtin_amdgcn_exp2f(m - mf), a1 = __builtin_amdgcn_exp2f(ml[0] - mf);
    const float inv = 1.0f / (a0 * l + a1 * ml[1]);
    const float c0 = a0 * inv, c1 = a1 * inv;
    f32x16 ea = g ? o2 : o0, eb = g ? o3 : o1;
    const unsigned xr = lds0 + 2 * SLOT + pw * 8192 + lane * 16;
#pragma unroll
    for (int i = 0; i < 4; ++i) {
      const f32x4 ua = *(const LAS f32x4*)(uintptr_t)(xr + i * 1024);
      const f32x4 ub = *(const LAS f32x4*)(uintptr_t)(xr + 4096 + i * 1024);
#pragma unroll
      for (int e = 0; e < 4; ++e) {
        ea[4 * i + e] = ea[4 * i + e] * c0 + ua[e] * c1;
        eb[4 * i + e] = eb[4 * i + e] * c0 + ub[e] * c1;
      }
    }
    uint16_t* op = ofrag + ((size_t)(((b * NH + h) * 4 + r) * 8 + 4 * g) * 512 + lane * 8);
    *(bf16x8*)(op) = pack8<0>(ea);
    *(bf16x8*)(op + 512) = pack8<1>(ea);
    *(bf16x8*)(op + 1024) = pack8<0>(eb);
    *(bf16x8*)(op + 1536) = pack8<1>(eb);
  }
}

__global__ __launch_bounds__(512, 2) void attn_kernel(const uint16_t* __restrict__ x, const uint16_t* __restrict__ kc,
                                                      const uint16_t* __restrict__ vc, const uint16_t* __restrict__ wq,
                                                      uint16_t* __restrict__ ofrag) {
  extern __shared__ __attribute__((aligned(16))) char smem[];
  const unsigned wid = rfl(threadIdx.x >> 6);
  const unsigned lds0 = rfl((unsigned)(uintptr_t)smem);
  unsigned long long pc;
  asm volatile("s_getpc_b64 %0" : "=s"(pc));
  if (wid >> 2) attn_body<1>(x, kc, vc, wq, ofrag, lds0, wid & 3, pc);
  else attn_body<0>(x, kc, vc, wq, ofrag, lds0, wid & 3, pc);
}

__global__ __launch_bounds__(512, 2) void oproj_kernel(const uint16_t* __restrict__ ofrag, const uint16_t* __restrict__ wo,
                                                       uint16_t* __restrict__ out) {
  extern __shared__ __attribute__((aligned(16))) char smem[];
  const unsigned tid = threadIdx.x, lane = tid & 63;
  const unsigned wid = rfl(tid >> 6);
  const unsigned l32 = lane & 31, hi = lane >> 5;
  const unsigned bid = blockIdx.x, xcd = bid & 7, idx = bid >> 3;
  const unsigned tm = 4 * (xcd & 3) + (idx & 3), tn = 8 * (xcd >> 2) + (idx >> 2);
  const unsigned b = tm >> 1, qb0 = 2 * (tm & 1);
  const unsigned lds0 = rfl((unsigned)(uintptr_t)smem);
  STAMP_DECL
  STAMP(0);
  const unsigned ring = lds0 + wid * 20480;

  const rsrc_t rsW = mkrsrc(wo + (size_t)wid * 512 * DM + tn * 64, 512 * DM * 2 - tn * 128);
  const rsrc_t rsO = mkrsrc(ofrag + (size_t)(b * NH + 4 * wid) * 4 * 8 * 512, 4 * 4 * 8 * 1024);
  const unsigned rw = lane >> 3;
  const unsigned voffW = rw * 2048 + 16 * ((lane & 7) ^ (((rw >> 1) & 1) << 2));
  const unsigned voffO = lane * 16 + qb0 * 8192;
  auto issue = [&](unsigned c, unsigned slot) {
    const unsigned d = ring + slot * 4096;
    dma<0>(d, voffW, rsW, c * 16 * 2048);
    dma<0>(d + 1024, voffW, rsW, c * 16 * 2048 + 8 * 2048);
    const unsigned so = (c >> 3) * 32768 + (c & 7) * 1024;
    dma<0>(d + 2048, voffO, rsO, so);
    dma<0>(d + 3072, voffO, rsO, so + 8192);
  };
  asm volatile("s_nop 4" ::: "memory");
  issue(0, 0); issue(1, 1); issue(2, 2); issue(3, 3);

  const unsigned q4 = (lane & 15) >> 2, p4 = lane & 3, blk = (lane >> 4) & 1;
  const unsigned baseA = ring + 128 * (4 * hi + q4) + 16 * (((q4 >> 1) << 2) + 2 * blk + (p4 >> 1)) + 8 * (p4 & 1);
  const unsigned baseB = ring + 2048 + lane * 16;
  f32x16 a00 = {}, a01 = {}, a10 = {}, a11 = {};
  auto comp = [&](unsigned slot) {
    const unsigned sa = baseA + slot * 4096, sb = baseB + slot * 4096;
    const bf16x8 w0 = cat(lds_tr(sa), lds_tr(sa + 1024));
    const bf16x8 w1 = cat(lds_tr(sa ^ 64), lds_tr((sa ^ 64) + 1024));
    const bf16x8 b0 = lds_b128(sb), b1 = lds_b128(sb + 1024);
    a00 = MFMA(w0, b0, a00); a01 = MFMA(w0, b1, a01);
    a10 = MFMA(w1, b0, a10); a11 = MFMA(w1, b1, a11);
  };
  STAMP(1);
  wait_vm<12>();
  STAMP(2);
  {
    unsigned slot = 0, fill = 4;
    for (unsigned c = 0; c < 28; ++c) {
      wait_vm<12>();
      issue(c + 4, fill);
      comp(slot);
      fill = slot;
      slot = (slot == 4) ? 0 : slot + 1;
    }
  }
  wait_vm<12>(); comp(28 % 5);
  wait_vm<8>();  comp(29 % 5);
  wait_vm<4>();  comp(30 % 5);
  wait_vm<0>();  comp(31 % 5);
  wait_vm<0>();
  STAMP(3);
  {
    const unsigned xw = ring + lane * 16;
#pragma unroll
    for (int i = 0; i < 4; ++i) {
      *(LAS f32x4*)(uintptr_t)(xw + i * 1024) = (f32x4){a00[4 * i], a00[4 * i + 1], a00[4 * i + 2], a00[4 * i + 3]};
      *(LAS f32x4*)(uintptr_t)(xw + 4096 + i * 1024) = (f32x4){a01[4 * i], a01[4 * i + 1], a01[4 * i + 2], a01[4 * i + 3]};
      *(LAS f32x4*)(uintptr_t)(xw + 8192 + i * 1024) = (f32x4){a10[4 * i], a10[4 * i + 1], a10[4 * i + 2], a10[4 * i + 3]};
      *(LAS f32x4*)(uintptr_t)(xw + 12288 + i * 1024) = (f32x4){a11[4 * i], a11[4 * i + 1], a11[4 * i + 2], a11[4 * i + 3]};
    }
    wait_bar<0>();
    const unsigned xr = lds0 + wid * 2048 + lane * 16;
    f32x4 u0 = {}, u1 = {};
#pragma unroll
    for (int w = 0; w < 8; ++w) {
      u0 += *(const LAS f32x4*)(uintptr_t)(xr + w * 20480);
      u1 += *(const LAS f32x4*)(uintptr_t)(xr + w * 20480 + 1024);
    }
    const unsigned nb = wid >> 2, qb = (wid >> 1) & 1;
    unsigned x0 = cvtpk(u0[0], u0[1]), x1 = cvtpk(u0[2], u0[3]), y0 = cvtpk(u1[0], u1[1]), y1 = cvtpk(u1[2], u1[3]);
    auto r0 = __builtin_amdgcn_permlane32_swap(x0, y0, false, false);
    auto r1 = __builtin_amdgcn_permlane32_swap(x1, y1, false, false);
    const u32x4 w4 = {r0[0], r1[0], r0[1], r1[1]};
    uint16_t* op = out + (size_t)(tm * 64 + qb * 32 + l32) * DM + tn * 64 + nb * 32 + 16 * (wid & 1) + 8 * hi;
    *(u32x4*)op = w4;
  }
}

extern "C" void kernel_launch(void* const* d_in, const int* in_sizes, int n_in, void* d_out, int out_size, void* d_ws,
                              size_t ws_size, hipStream_t stream) {
  const uint16_t* x = (const uint16_t*)d_in[0];
  const uint16_t* kc = (const uint16_t*)d_in[1];
  const uint16_t* vc = (const uint16_t*)d_in[2];
  const uint16_t* wq = (const uint16_t*)d_in[3];
  const uint16_t* wo = (const uint16_t*)d_in[4];
  uint16_t* ofrag = (uint16_t*)d_ws;
  (void)hipFuncSetAttribute((const void*)attn_kernel, hipFuncAttributeMaxDynamicSharedMemorySize, LDS1);
  (void)hipFuncSetAttribute((const void*)oproj_kernel, hipFuncAttributeMaxDynamicSharedMemorySize, LDS2);
  hipLaunchKernelGGL(attn_kernel, dim3(256), dim3(512), LDS1, stream, x, kc, vc, wq, ofrag);
  hipLaunchKernelGGL(oproj_kernel, dim3(256), dim3(512), LDS2, stream, (const uint16_t*)ofrag, wo, (uint16_t*)d_out);
}
```

```cpp
#include <hip/hip_runtime.h>
#include <hip/hip_bf16.h>
#include <stdint.h>
#include <utility>
#include <type_traits>

typedef float f32x16 __attribute__((ext_vector_type(16)));
typedef float f32x4 __attribute__((ext_vector_type(4)));
typedef float f32x2 __attribute__((ext_vector_type(2)));
typedef __bf16 bf16x8 __attribute__((ext_vector_type(8)));
typedef __bf16 bf16x2 __attribute__((ext_vector_type(2)));
typedef short s16x4 __attribute__((ext_vector_type(4)));
typedef unsigned u32x4 __attribute__((ext_vector_type(4)));
typedef unsigned u32x2 __attribute__((ext_vector_type(2)));
typedef unsigned long long u64;
typedef __amdgpu_buffer_rsrc_t rsrc_t;
typedef __attribute__((address_space(1))) u64 gu64;

#define LAS __attribute__((address_space(3)))
#define DI __device__ __forceinline__
#define MFMA(a, b, c) __builtin_amdgcn_mfma_f32_32x32x16_bf16((a), (b), (c), 0, 0, 0)
#define SBAR() __builtin_amdgcn_sched_barrier(0)
#define PIN(x) asm volatile("" : "+v"(x))
#define BF(x) __builtin_bit_cast(bf16x8, x)
#define IC(n) std::integral_constant<int, (n)>{}

constexpr int SQ = 128, DM = 1024, NH = 32, SK = 2048, DH = 128;
constexpr int NT = SK / 64;
constexpr unsigned HB = 16384;
constexpr int LDSB = 10 * HB;
constexpr float CEXP = 0.12751743082459868f;
constexpr float SUMLIM = 1024.0f;
constexpr unsigned WS_BATCH = 4u << 20, WS_FLAG = 1u << 20, WS_PART = WS_FLAG + 65536, WS_IDLE = 3u << 20;
constexpr unsigned SPIN_CAP = 1u << 18;

#define XPOL 0
#define WPOL 0
#define OPOL 0
#define HK0 12
#define HK1 12
#define HK2 13
#define HV0 13
#define HV1 14
#define TMASK 32
#define TSTEP 0
#define PF0 2
#define PFD 2
#define PFPOL ""
#define TOUCH 1

DI unsigned rfl(unsigned x) { return __builtin_amdgcn_readfirstlane(x); }
DI rsrc_t mkrsrc(const void* p, unsigned bytes) { return __builtin_amdgcn_make_buffer_rsrc((void*)p, (short)0, (int)bytes, 0x00020000); }
template <int N> DI void wait_bar() { asm volatile("s_waitcnt vmcnt(%0) lgkmcnt(0)\n\ts_barrier" ::"i"(N) : "memory"); }
DI void lbar() { asm volatile("s_waitcnt lgkmcnt(0)\n\ts_barrier" ::: "memory"); }
DI bf16x8 lds_b128(unsigned addr) { return *(const LAS bf16x8*)(uintptr_t)addr; }
DI f32x4 lds_f4(unsigned addr) { return *(const LAS f32x4*)(uintptr_t)addr; }
DI void sts_f4(unsigned addr, f32x4 v) { *(LAS f32x4*)(uintptr_t)addr = v; }
DI s16x4 lds_tr(unsigned addr) { return __builtin_amdgcn_ds_read_tr16_b64_v4i16((LAS s16x4*)(uintptr_t)addr); }
DI bf16x8 cat(s16x4 a, s16x4 b) { return __builtin_bit_cast(bf16x8, __builtin_shufflevector(a, b, 0, 1, 2, 3, 4, 5, 6, 7)); }
DI unsigned cvtpk(float lo, float hi) {
  f32x2 v = {lo, hi};
  return __builtin_bit_cast(unsigned, __builtin_convertvector(v, bf16x2));
}
template <int S> DI u32x4 pack8(const f32x16& v) {
  return (u32x4){cvtpk(v[8 * S + 0], v[8 * S + 1]), cvtpk(v[8 * S + 2], v[8 * S + 3]), cvtpk(v[8 * S + 4], v[8 * S + 5]), cvtpk(v[8 * S + 6], v[8 * S + 7])};
}
template <int S> DI f32x4 quad(const f32x16& v) { return (f32x4){v[4 * S], v[4 * S + 1], v[4 * S + 2], v[4 * S + 3]}; }
DI float swap_max(float v) {
  auto rr = __builtin_amdgcn_permlane32_swap(__float_as_uint(v), __float_as_uint(v), false, false);
  return fmaxf(__uint_as_float(rr[0]), __uint_as_float(rr[1]));
}
DI float swap_sum(float v) {
  auto rr = __builtin_amdgcn_permlane32_swap(__float_as_uint(v), __float_as_uint(v), false, false);
  return __uint_as_float(rr[0]) + __uint_as_float(rr[1]);
}
#define DMA4(pol) asm volatile("s_mov_b32 m0, %0\n\ts_nop 0\n\t" \
    "buffer_load_dwordx4 %1, %5, %6 offen" pol " lds\n\t" \
    "buffer_load_dwordx4 %2, %5, %7 offen offset:1024" pol " lds\n\t" \
    "buffer_load_dwordx4 %3, %5, %8 offen offset:2048" pol " lds\n\t" \
    "buffer_load_dwordx4 %4, %5, %9 offen offset:3072" pol " lds" \
    ::"s"(dst), "v"(va), "v"(vb), "v"(vc_), "v"(vd), "s"(rs), "s"(s0), "s"(s1), "s"(s2), "s"(s3) : "memory")
template <int POL>
DI void dma4(unsigned dst, unsigned va, unsigned vb, unsigned vc_, unsigned vd, rsrc_t rs, unsigned s0, unsigned s1, unsigned s2, unsigned s3) {
  if constexpr (POL == 1) DMA4(" nt");
  else if constexpr (POL == 2) DMA4(" sc1");
  else DMA4("");
}
template <class F, int... Is> DI void sfor_impl(F&& f, std::integer_sequence<int, Is...>) { (f(std::integral_constant<int, Is>{}), ...); }
template <int N, class F> DI void sfor(F&& f) { sfor_impl(f, std::make_integer_sequence<int, N>{}); }
DI void poll(const gu64* p, u64 tag) {
#pragma unroll 1
  for (unsigned n = 0; n < SPIN_CAP; ++n) {
    const u64 v = __hip_atomic_load(p, __ATOMIC_RELAXED, __HIP_MEMORY_SCOPE_AGENT);
    if (__all(v == tag)) break;
    __builtin_amdgcn_s_sleep(2);
  }
}

constexpr int hsite(int G, int k) { return G ? (k == 0 ? HV0 : k == 1 ? HV1 : 99) : (k == 0 ? HK0 : k == 1 ? HK1 : k == 2 ? HK2 : k == 3 ? 16 : 99); }
constexpr int pfsite(int it) { return PF0 >= 0 && it >= PF0 && (it - PF0) % PFD == 0 && (it - PF0) / PFD < 4 ? (it - PF0) / PFD : -1; }
constexpr int hyoung(int G, int id, int upto) {
  int seq[64] = {}, cnt[64] = {}, n = 0;
  seq[n] = 0; cnt[n++] = 4; seq[n] = 1; cnt[n++] = 4;
  seq[n] = 90; cnt[n++] = G ? (TOUCH ? 1 : 0) : (TOUCH ? 2 : 1);
  for (int it = -1; it < upto; ++it) {
    if (it + 3 <= 15) { seq[n] = it + 3; cnt[n++] = 4; }
    if (pfsite(it) >= 0) { seq[n] = 91; cnt[n++] = 1; }
    for (int k = 0; k < 4; ++k) if (hsite(G, k) == it) { seq[n] = 16 + k; cnt[n++] = 4; }
  }
  int y = 0;
  for (int i = n - 1; i >= 0; --i) { if (seq[i] == id) return y; y += cnt[i]; }
  return -1;
}
static_assert(HK0 >= 1 && HK0 <= HK1 && HK1 <= HK2 && HK2 >= 13 && HK2 <= 14 && HV0 <= HV1 && HV1 <= 15, "head schedule");
constexpr unsigned qblock(int G, int i) { return ((i + 2) % 3 == 0 ? (G ? 5 : 2) : (i + 2) % 3 == 1 ? 6 + G : 8 + G) * HB; }

struct QFrag { bf16x8 xb[4]; s16x4 wa[4][2], wb[4][2]; };

#define IT(i)
#define ITL(i)

template <int G>
DI void body(const uint16_t* __restrict__ x, const uint16_t* __restrict__ kc, const uint16_t* __restrict__ vc, const uint16_t* __restrict__ wq,
             const uint16_t* __restrict__ wo, uint16_t* __restrict__ out, char* __restrict__ ws, unsigned lds0, unsigned r) {
  constexpr unsigned g = G;
  const unsigned lane = threadIdx.x & 63, wid = 4 * g + r;
  const unsigned l32 = lane & 31, hi = lane >> 5;
  const unsigned bid = blockIdx.x, xcd = bid & 7, idx = bid >> 3;
  const unsigned h = 4 * xcd + (idx & 3), b = idx >> 2;
  IT(0);

  const unsigned l4 = lane >> 4;
  const size_t kvoff = (size_t)(b * NH + h) * SK * DH;
  const rsrc_t rsKV = mkrsrc((g ? vc : kc) + kvoff, SK * DH * 2);
  const rsrc_t rsQ = g ? mkrsrc(wq + h * DH, DM * NH * DH * 2 - h * DH * 2) : mkrsrc(x + (size_t)b * SQ * DM, SQ * DM * 2);
  const unsigned voffA = (16 * r + l4) * 256 + 16 * ((lane & 15) ^ (g ? (l4 << 2) : (l4 | ((r & 1) << 3))));
  const unsigned voffB = g ? voffA : voffA ^ 64;
  const unsigned voffQ = g ? (16 * r + l4) * 8192 + 16 * ((lane & 15) ^ (l4 << 2))
                           : (32 * r + (lane >> 3)) * 2048 + 16 * ((lane & 7) ^ l4);
  const unsigned voffQ1 = g ? voffQ : voffQ ^ 64;
  constexpr unsigned qstS = g ? 64 * 8192 : 128, qstJ = (g ? 4 * 8192 : 8 * 2048) - 1024;
  const unsigned dstw = lds0 + r * 4096;
  const unsigned kstart = (2 * b + 4 * (h & 3)) & 15;
  auto issueQ = [&](unsigned i, unsigned blk_) {
    const unsigned so = ((i + kstart) & 15) * qstS;
    dma4<g ? WPOL : XPOL>(dstw + blk_, voffQ, voffQ1, voffQ, voffQ1, rsQ, so, so + qstJ, so + 2 * qstJ, so + 3 * qstJ);
  };
  auto issueKV = [&](unsigned so, int t3) {
    dma4<1>(dstw + (3 * g + t3) * HB, voffA, voffA, voffB, voffB, rsKV, so, so, so, so);
  };

  asm volatile("s_nop 4" ::: "memory");
  issueQ(0, qblock(G, 0)); issueQ(1, qblock(G, 1));

  const unsigned ks = h >> 3, nt = h & 7, oq = (ks + nt) & 3;
  char* const wsb = ws + (size_t)b * WS_BATCH;
  const rsrc_t rsB = mkrsrc(wsb, WS_BATCH);
  const rsrc_t rsO = mkrsrc(out + (size_t)(b * SQ + 32 * oq) * DM, 32 * DM * 2);
  if constexpr (!G) {
    const unsigned ve = lane < 2 ? WS_FLAG + 4 * lane : WS_IDLE;
    asm volatile("s_mov_b32 m0, %0\n\ts_nop 0\n\tbuffer_load_dword %1, %2, 0 offen sc1 lds" ::"s"(dstw), "v"(ve), "s"(rsB) : "memory");
    if (TOUCH) asm volatile("s_mov_b32 m0, %0\n\ts_nop 0\n\tbuffer_load_dword off, %1, 0 lds" ::"s"(dstw + 256), "s"(rsO) : "memory");
  } else if (TOUCH) {
    const rsrc_t rsT = mkrsrc(wo + (size_t)ks * 1024 * DM, 1024 * DM * 2);
    asm volatile("s_mov_b32 m0, %0\n\ts_nop 0\n\tbuffer_load_dword %1, %2, 0 offen lds" ::"s"(dstw + 3 * HB), "v"((lane & TMASK) * 32768 + r * TSTEP), "s"(rsT) : "memory");
  }

  const unsigned q4 = (lane & 15) >> 2, p4 = lane & 3, blk = (lane >> 4) & 1;
  const unsigned xrow = 32 * r + l32;
  const unsigned baseX = lds0 + 128 * xrow + 16 * (hi ^ ((xrow >> 1) & 7));
  const unsigned baseW = lds0 + 256 * (8 * hi + q4) + 16 * (4 * ((2 * g) ^ q4) + 2 * blk + (p4 & 1)) + 8 * (p4 >> 1);
  const unsigned baseK = lds0 + 8192 * g + 256 * l32 + 16 * (hi ^ ((l32 & 3) | ((l32 >> 3) << 2)));
  const unsigned baseT = lds0 + 256 * (4 * hi + q4) + 16 * (4 * q4 + 2 * blk + (p4 >> 1)) + 8 * (p4 & 1);

  f32x16 qa0 = {}, qa1 = {};
  QFrag fr[2];
  auto qiter = [&](QFrag& cur, QFrag& nxt, unsigned bx, unsigned bw, bool mm) {
    unsigned sx = baseX + bx, sw0 = baseW + bw;
    PIN(sx); PIN(sw0);
    const unsigned sw1 = sw0 ^ 64;
#pragma unroll
    for (int s = 0; s < 4; ++s) {
      nxt.xb[s] = lds_b128(sx ^ (s << 5)); nxt.wa[s][0] = lds_tr(sw0 + 4096 * s); nxt.wa[s][1] = lds_tr(sw0 + 4096 * s + 1024);
      SBAR();
      if (mm) { qa0 = MFMA(cat(cur.wa[s][0], cur.wa[s][1]), cur.xb[s], qa0); PIN(qa0); }
      SBAR();
      nxt.wb[s][0] = lds_tr(sw1 + 4096 * s); nxt.wb[s][1] = lds_tr(sw1 + 4096 * s + 1024);
      SBAR();
      if (mm) { qa1 = MFMA(cat(cur.wb[s][0], cur.wb[s][1]), cur.xb[s], qa1); PIN(qa1); }
      SBAR();
    }
  };
  auto hslot = [&](auto ic) {
    constexpr int it = decltype(ic)::value;
    if constexpr (it + 3 <= 15) issueQ(it + 3, qblock(G, it + 3));
    if constexpr (pfsite(it) >= 0) {
      const rsrc_t rsT = mkrsrc(wo + (size_t)(ks * 1024 + idx * 32 + wid * 4 + pfsite(it)) * DM + (nt >> 2) * 512, 1024);
      asm volatile("s_mov_b32 m0, %0\n\ts_nop 0\n\tbuffer_load_dwordx4 %1, %2, 0 offen" PFPOL " lds" ::"s"(dstw + (3 * g + 1) * HB), "v"(lane * 16), "s"(rsT) : "memory");
    }
    sfor<4>([&](auto kc_) {
      constexpr int k = decltype(kc_)::value;
      if constexpr (hsite(G, k) == it) issueKV(k * HB, k % 3);
    });
  };
  u64 tag = 0;
  wait_bar<hyoung(G, 0, -1)>();
  IT(1);
  qiter(fr[1], fr[0], qblock(0, 0), qblock(1, 0), false);
  hslot(IC(-1));
  sfor<15>([&](auto ic) {
    constexpr int i = decltype(ic)::value;
    wait_bar<hyoung(G, i + 1, i)>();
    ITL(i + 2);
    if constexpr (G) hslot(IC(i));
    if constexpr (!G && i == 1) {
      const u32x2 e = *(const LAS u32x2*)(uintptr_t)dstw;
      tag = (((u64)rfl(e[1]) << 32) | rfl(e[0])) + 0x9E3779B97F4A7C15ull;
      asm volatile("" : "+s"(tag));
    }
    qiter(fr[i & 1], fr[(i + 1) & 1], qblock(0, i + 1), qblock(1, i + 1), true);
    if constexpr (!G) hslot(IC(i));
  });
  bf16x8 qf[8], kf[8];
  unsigned aK[8], aV[4];
  {
    QFrag& cur = fr[1];
    const unsigned xw = lds0 + 6 * HB + wid * 4096 + lane * 16;
#pragma unroll
    for (int s = 0; s < 4; ++s) qa0 = MFMA(cat(cur.wa[s][0], cur.wa[s][1]), cur.xb[s], qa0);
    PIN(qa0);
    qa1 = MFMA(cat(cur.wb[0][0], cur.wb[0][1]), cur.xb[0], qa1);
    qa1 = MFMA(cat(cur.wb[1][0], cur.wb[1][1]), cur.xb[1], qa1); PIN(qa1);
    SBAR();
#pragma unroll
    for (int k = 0; k < 8; ++k) { aK[k] = baseK ^ (k << 5); PIN(aK[k]); }
#pragma unroll
    for (int k = 0; k < 4; ++k) { aV[k] = (baseT + 3 * HB + 8192 * g) ^ (k << 6); PIN(aV[k]); }
    qa0 *= CEXP;
    qf[4 * g + 0] = BF(pack8<0>(qa0)); PIN(qf[4 * g + 0]);
    SBAR();
    qa1 = MFMA(cat(cur.wb[2][0], cur.wb[2][1]), cur.xb[2], qa1); PIN(qa1);
    SBAR();
    qf[4 * g + 1] = BF(pack8<1>(qa0)); PIN(qf[4 * g + 1]);
    SBAR();
    qa1 = MFMA(cat(cur.wb[3][0], cur.wb[3][1]), cur.xb[3], qa1); PIN(qa1);
    SBAR();
    *(LAS bf16x8*)(uintptr_t)(xw) = qf[4 * g + 0];
    *(LAS bf16x8*)(uintptr_t)(xw + 1024) = qf[4 * g + 1];
    qa1 *= CEXP;
    qf[4 * g + 2] = BF(pack8<0>(qa1)); qf[4 * g + 3] = BF(pack8<1>(qa1));
    *(LAS bf16x8*)(uintptr_t)(xw + 2048) = qf[4 * g + 2];
    *(LAS bf16x8*)(uintptr_t)(xw + 3072) = qf[4 * g + 3];
    if constexpr (G) lbar(); else wait_bar<hyoung(0, 16, 15)>();
    IT(17);
    hslot(IC(15));
    const unsigned xr = lds0 + 6 * HB + (wid ^ 4) * 4096 + lane * 16;
#pragma unroll
    for (int k = 0; k < 4; ++k) kf[4 * g + k] = lds_b128(aK[4 * g + k]);
#pragma unroll
    for (int k = 0; k < 4; ++k) { qf[4 * (1 - g) + k] = lds_b128(xr + k * 1024); kf[4 * (1 - g) + k] = lds_b128(aK[4 * (1 - g) + k]); }
  }

  f32x16 o0, o1, o2, o3, sc, negm;
  s16x4 va[8], vb[8];
  u32x4 pa0, pa1, pb0, pb1;
  float m, l;

  auto rowmax = [&](const f32x16& s) {
    float a = fmaxf(fmaxf(s[0], s[1]), s[2]), c = fmaxf(fmaxf(s[3], s[4]), s[5]);
    a = fmaxf(fmaxf(a, s[6]), s[7]); c = fmaxf(fmaxf(c, s[8]), s[9]);
    a = fmaxf(fmaxf(a, s[10]), s[11]); c = fmaxf(fmaxf(c, s[12]), s[13]);
    a = fmaxf(fmaxf(a, s[14]), s[15]);
    return swap_max(fmaxf(a, c));
  };
  auto probs = [&](u32x4& p0, u32x4& p1) {
    float sacc = 0.f;
#pragma unroll
    for (int i = 0; i < 8; ++i) {
      const float e0 = __builtin_amdgcn_exp2f(sc[2 * i]), e1 = __builtin_amdgcn_exp2f(sc[2 * i + 1]);
      sacc += e0; sacc += e1;
      if (i < 4) p0[i] = cvtpk(e0, e1); else p1[i - 4] = cvtpk(e0, e1);
    }
    return sacc;
  };
  auto guard = [&](float sacc, u32x4& p0, u32x4& p1) {
    if (__builtin_expect(__any(!(sacc <= SUMLIM)), 0)) {
      const float dl = fmaxf(rowmax(sc), 0.f);
      const float alpha = __builtin_amdgcn_exp2f(-dl);
      m += dl;
      l *= alpha;
      o0 *= alpha; o1 *= alpha; o2 *= alpha; o3 *= alpha;
      sc -= dl; negm -= dl;
      sacc = probs(p0, p1);
      PIN(m); PIN(l); PIN(sacc); PIN(p0); PIN(p1); PIN(negm); PIN(o0); PIN(o1); PIN(o2); PIN(o3);
    }
    l += sacc;
  };
  auto phaseA = [&](unsigned vo) {
    __builtin_amdgcn_s_setprio(1);
#pragma unroll
    for (int k = 0; k < 8; ++k) {
      const unsigned a_ = aV[k & 3] + vo + 4096 * (k >> 2);
      va[k] = lds_tr(a_); vb[k] = lds_tr(a_ + 2048);
      SBAR();
      sc = MFMA(kf[k], qf[k], k == 0 ? negm : sc); PIN(sc);
      SBAR();
    }
  };
  auto phaseB = [&](const u32x4& po0, const u32x4& po1, u32x4& pn0, u32x4& pn1, auto&& rd) {
    float sacc = 0.f;
    __builtin_amdgcn_s_setprio(0);
    SBAR();
#pragma unroll
    for (int i = 0; i < 8; ++i) {
      rd(i);
      SBAR();
      const bf16x8 vf = cat(va[i], vb[i]);
      const bf16x8 pf = i < 4 ? BF(po0) : BF(po1);
      if ((i & 3) == 0) { o0 = MFMA(vf, pf, o0); PIN(o0); }
      if ((i & 3) == 1) { o1 = MFMA(vf, pf, o1); PIN(o1); }
      if ((i & 3) == 2) { o2 = MFMA(vf, pf, o2); PIN(o2); }
      if ((i & 3) == 3) { o3 = MFMA(vf, pf, o3); PIN(o3); }
      SBAR();
      const float e0 = __builtin_amdgcn_exp2f(sc[2 * i]), e1 = __builtin_amdgcn_exp2f(sc[2 * i + 1]);
      if (i == 0) sacc = e0 + e1; else { sacc += e0; sacc += e1; }
      if (i < 4) { pn0[i] = cvtpk(e0, e1); PIN(pn0); } else { pn1[i - 4] = cvtpk(e0, e1); PIN(pn1); }
      PIN(sacc);
      SBAR();
    }
    asm volatile("" ::: "memory");
    return sacc;
  };

  {
    sc = (f32x16){};
#pragma unroll
    for (int k = 0; k < 4; ++k) sc = MFMA(kf[4 * g + k], qf[4 * g + k], sc);
#pragma unroll
    for (int k = 0; k < 4; ++k) sc = MFMA(kf[4 * (1 - g) + k], qf[4 * (1 - g) + k], sc);
    PIN(sc);
    SBAR();
    o0 = (f32x16){}; o1 = (f32x16){}; o2 = (f32x16){}; o3 = (f32x16){};
    PIN(o0); PIN(o1); PIN(o2); PIN(o3);
    SBAR();
    m = rowmax(sc);
#pragma unroll
    for (int i = 0; i < 16; ++i) { negm[i] = -m; sc[i] -= m; }
    PIN(negm); PIN(sc);
    if constexpr (G) lbar(); else wait_bar<hyoung(0, 17, 16)>();
    hslot(IC(16));
#pragma unroll
    for (int k = 0; k < 8; ++k) kf[k] = lds_b128(aK[k] + HB);
    l = probs(pa0, pa1);
    PIN(pa0); PIN(pa1); PIN(l);
    wait_bar<hyoung(G, G ? 16 : 18, 17)>();
    IT(18);
  }

  const rsrc_t rsW = mkrsrc(wo + (size_t)(ks * 1024 + g * 512) * DM + nt * 128, 512 * DM * 2 - nt * 256);
  s16x4 wfa[8][4], wfb[8][4];
  auto issueW = [&](unsigned p, unsigned blk_) {
    const unsigned vw = (16 * r + l4) * 2048 + 16 * ((lane & 15) ^ (l4 << 2));
    const unsigned so = ((p + b) & 7) * (64 * 2048);
    dma4<OPOL>(dstw + blk_ * HB, vw, vw, vw, vw, rsW, so, so + 7168, so + 2 * 7168, so + 3 * 7168);
  };
  auto readW = [&](int p, int k, unsigned blk_) {
    const unsigned a_ = (baseT ^ (r << 6)) + blk_ * HB + 4096 * k;
    wfa[p][k] = lds_tr(a_); wfb[p][k] = lds_tr(a_ + 2048);
  };

  unsigned nso = (g ? 2 : 4) * HB;
  auto step = [&](auto jc, auto lastc) {
    constexpr int j = decltype(jc)::value;
    constexpr bool last = decltype(lastc)::value;
    constexpr bool kv = !last || G || j <= 28;
    constexpr int wpos = !last ? -1 : G ? (j == 26 ? 0 : j == 28 ? 1 : -1) : (j == 25 ? 0 : j == 27 ? 1 : j == 30 ? 2 : -1);
    constexpr int wblk = wpos == 2 ? 0 : 6 + 2 * wpos + G;
    auto issue = [&]() {
      if constexpr (kv) { issueKV(nso, (G ? j + 1 : j) % 3); nso += HB; }
      if constexpr (wpos >= 0) issueW(wpos, wblk);
    };
    u32x4& po0 = (j & 1) ? pa0 : pb0; u32x4& po1 = (j & 1) ? pa1 : pb1;
    u32x4& pn0 = (j & 1) ? pb0 : pa0; u32x4& pn1 = (j & 1) ? pb1 : pa1;
    if constexpr (G) issue();
    phaseA(((j + 2) % 3) * HB);
    const float sacc = phaseB(po0, po1, pn0, pn1, [&](int i) {
      if (i < 4) { kf[2 * i] = lds_b128(aK[2 * i] + ((j + 1) % 3) * HB); kf[2 * i + 1] = lds_b128(aK[2 * i + 1] + ((j + 1) % 3) * HB); }
    });
    if constexpr (!G) issue();
    guard(sacc, pn0, pn1);
    wait_bar<(kv ? 4 : 0) + (wpos >= 0 ? 4 : 0)>();
  };
  for (unsigned jj = 0; jj < 4; ++jj)
    sfor<6>([&](auto c) { step(IC(decltype(c)::value + 1), std::false_type{}); ITL(19 + 6 * jj + decltype(c)::value); });
  sfor<6>([&](auto c) { step(IC(decltype(c)::value + 25), std::true_type{}); ITL(43 + decltype(c)::value); });
  IT(48);

  {
    if constexpr (G) issueW(2, 1);
    phaseA(0);
    const float sacc = phaseB(pa0, pa1, pb0, pb1, [&](int i) { readW(i >> 2, i & 3, 6 + 2 * (i >> 2) + g); });
    guard(sacc, pb0, pb1);
    l = swap_sum(l);
    const unsigned xl = lds0 + lane * 16;
    *(LAS u32x4*)(uintptr_t)(xl + 2 * HB + wid * 2048) = pb0;
    *(LAS u32x4*)(uintptr_t)(xl + 2 * HB + wid * 2048 + 1024) = pb1;
    *(LAS f32x2*)(uintptr_t)(lds0 + 5 * HB + wid * 512 + lane * 8) = (f32x2){m, l};
    lbar();
    IT(49);
    {
      const unsigned xw = xl + 6 * HB + wid * 8192;
      const f32x16 sa = g ? o0 : o2, sb = g ? o1 : o3;
      sts_f4(xw, quad<0>(sa)); sts_f4(xw + 1024, quad<1>(sa)); sts_f4(xw + 2048, quad<2>(sa)); sts_f4(xw + 3072, quad<3>(sa));
      sts_f4(xw + 4096, quad<0>(sb)); sts_f4(xw + 5120, quad<1>(sb)); sts_f4(xw + 6144, quad<2>(sb)); sts_f4(xw + 7168, quad<3>(sb));
    }
    const unsigned pw = wid ^ 4;
    const bf16x8 pp0 = lds_b128(xl + 2 * HB + pw * 2048), pp1 = lds_b128(xl + 2 * HB + pw * 2048 + 1024);
    const f32x2 ml = *(const LAS f32x2*)(uintptr_t)(lds0 + 5 * HB + pw * 512 + lane * 8);
    const float mf = fmaxf(m, ml[0]);
    const float a0 = __builtin_amdgcn_exp2f(m - mf), a1 = __builtin_amdgcn_exp2f(ml[0] - mf);
    const float inv = 1.0f / (a0 * l + a1 * ml[1]);
    float c0 = a0 * inv, c1 = a1 * inv;
    PIN(c0); PIN(c1);
    wait_bar<0>();
    IT(50);
#pragma unroll
    for (int k = 0; k < 8; ++k) {
      const unsigned a_ = aV[2 * g + (k & 1)] + HB + 4096 * ((k >> 1) & 1) + (k >> 2) * (g ? -8192 : 8192);
      va[k] = lds_tr(a_); vb[k] = lds_tr(a_ + 2048);
    }
    f32x16 ea = g ? o2 : o0, eb = g ? o3 : o1, ua, ub;
    const unsigned xr = xl + 6 * HB + pw * 8192;
#pragma unroll
    for (int i = 0; i < 4; ++i) {
      const f32x4 t0 = lds_f4(xr + i * 1024), t1 = lds_f4(xr + 4096 + i * 1024);
#pragma unroll
      for (int e = 0; e < 4; ++e) { ua[4 * i + e] = t0[e]; ub[4 * i + e] = t1[e]; }
    }
#pragma unroll
    for (int k = 0; k < 4; ++k) readW(2, k, g);
    SBAR();
    ea = MFMA(cat(va[0], vb[0]), BF(pb0), ea); eb = MFMA(cat(va[1], vb[1]), BF(pb0), eb);
    ea = MFMA(cat(va[2], vb[2]), BF(pb1), ea); eb = MFMA(cat(va[3], vb[3]), BF(pb1), eb);
    ua = MFMA(cat(va[4], vb[4]), pp0, ua); ub = MFMA(cat(va[5], vb[5]), pp0, ub);
    ua = MFMA(cat(va[6], vb[6]), pp1, ua); ub = MFMA(cat(va[7], vb[7]), pp1, ub);
    PIN(ea); PIN(eb); PIN(ua); PIN(ub);
    SBAR();
    lbar();
    issueW(3, g); issueW(4, 2 + g);
    SBAR();
    ea = ea * c0 + ua * c1;
    const unsigned vo = ((h * 4 + r) * 8 + 4 * g) * 1024 + lane * 16;
    __builtin_amdgcn_raw_buffer_store_b128(pack8<0>(ea), rsB, vo, 0, 16);
    __builtin_amdgcn_raw_buffer_store_b128(pack8<1>(ea), rsB, vo + 1024, 0, 16);
    eb = eb * c0 + ub * c1;
    __builtin_amdgcn_raw_buffer_store_b128(pack8<0>(eb), rsB, vo + 2048, 0, 16);
    __builtin_amdgcn_raw_buffer_store_b128(pack8<1>(eb), rsB, vo + 3072, 0, 16);
    IT(51);
    wait_bar<0>();
    IT(52);
  }
  gu64* const flag = (gu64*)(wsb + WS_FLAG);
  if constexpr (!G) if (r == 0 && lane == 0) __hip_atomic_store(flag + 32 * (1 + h), tag, __ATOMIC_RELAXED, __HIP_MEMORY_SCOPE_AGENT);
  issueW(5, 4 + g); issueW(6, 6 + g); issueW(7, 8 + g);
#pragma unroll
  for (int k = 0; k < 8; ++k) readW(3 + (k >> 2), k & 3, 2 * (k >> 2) + g);
  asm volatile("" ::: "memory");
  if constexpr (!G) if (r == 0) poll(flag + 32 * (1 + 8 * ks + (lane & 7)), tag);
  wait_bar<0>();
  IT(53);

  auto issueD = [&](unsigned s) {
    const unsigned c = (4 * (s & 1) + r + b) & 7, q = (oq + 1 + (s >> 1)) & 3;
    const unsigned so = (8 * ks + 4 * g + (c >> 1)) * 32768 + q * 8192 + (c & 1) * 4096;
    const unsigned vl = lane * 16;
    dma4<2>(dstw + (2 * (s & 3) + g) * HB, vl, vl, vl, vl, rsB, so, so, so, so);
  };
  issueD(0); issueD(1);
#pragma unroll
  for (int k = 0; k < 12; ++k) readW(5 + (k >> 2), k & 3, 4 + 2 * (k >> 2) + g);
  wait_bar<4>();
  IT(54);
  issueD(2); issueD(3);
  {
    unsigned aD[2] = {lds0 + g * HB + lane * 16, lds0 + (4 + g) * HB + lane * 16};
    PIN(aD[0]); PIN(aD[1]);
    unsigned aR = lds0 + 8 * HB + r * 4096 + lane * 16;
    PIN(aR);
    bf16x8 of[8];
    f32x16 acc[2];
    f32x4 k0, k1, t0, t1;
    auto rdO = [&](int f) { of[f & 7] = lds_b128(aD[(f >> 5) & 1] + ((f >> 4) & 1) * 2 * HB + (f & 15) * 1024); };
    auto give = [&](int z) {
      const f32x16& a = acc[z & 1];
      sts_f4(aR + (z & 1) * HB + g * 2048, quad<2 - 2 * G>(a)); sts_f4(aR + (z & 1) * HB + g * 2048 + 1024, quad<3 - 2 * G>(a));
      k0 = quad<2 * G>(a); k1 = quad<2 * G + 1>(a);
    };
    auto take = [&](int z) {
      t0 = lds_f4(aR + (z & 1) * HB + (1 - g) * 2048); t1 = lds_f4(aR + (z & 1) * HB + (1 - g) * 2048 + 1024);
    };
    auto send = [&](int z) {
      const unsigned vo = WS_PART + ((nt * 4 + ((ks + 1 + z) & 3)) * 3 + z) * HB + wid * 2048 + lane * 16;
      k0 += t0; k1 += t1;
      __builtin_amdgcn_raw_buffer_store_b128(__builtin_bit_cast(u32x4, k0), rsB, vo, 0, 16);
      __builtin_amdgcn_raw_buffer_store_b128(__builtin_bit_cast(u32x4, k1), rsB, vo + 1024, 0, 16);
      asm volatile("s_nop 1" ::: "memory");
    };
#pragma unroll
    for (int f = 0; f < 6; ++f) rdO(f);
    sfor<8>([&](auto sc_) {
      constexpr int s = decltype(sc_)::value, z = s >> 1;
      sfor<16>([&](auto ic) {
        constexpr int i = decltype(ic)::value, f = 16 * s + i;
        if constexpr (i <= 9) rdO(f + 6);
        if constexpr (i == 2 && (s == 2 || s == 4 || s == 6)) give(z - 1);
        if constexpr (i == 4 && s == 6) { wait_bar<0>(); take(2); }
        if constexpr (i == 6 && s == 6) send(2);
        if constexpr (i == 8 && s == 7) {
          wait_bar<0>();
          if constexpr (!G) if (r == 0 && lane == 0) __hip_atomic_store(flag + 32 * (33 + h), tag, __ATOMIC_RELAXED, __HIP_MEMORY_SCOPE_AGENT);
        }
        if constexpr (i == 13 && s < 7) {
          if constexpr (s < 6) wait_bar<(s < 5 ? 8 : 4)>();
          if constexpr (s < 4) issueD(s + 4);
          rdO(f + 3); rdO(f + 4); rdO(f + 5);
          if constexpr (s == 2 || s == 4) take(z - 1);
        }
        if constexpr (i == 14 && s < 7) { rdO(f + 5); rdO(f + 6); }
        if constexpr (i == 15 && s < 7) { rdO(f + 6); if constexpr (s == 2 || s == 4) send(z - 1); }
        SBAR();
        constexpr int p = 4 * (s & 1) + (i >> 2), k = i & 3;
        if constexpr (i == 0 && !(s & 1)) acc[z & 1] = MFMA(cat(wfa[p][k], wfb[p][k]), of[f & 7], (f32x16){});
        else acc[z & 1] = MFMA(cat(wfa[p][k], wfb[p][k]), of[f & 7], acc[z & 1]);
        PIN(acc[z & 1]);
        SBAR();
      });
      if constexpr (s & 1) IT(55 + z);
    });
    give(3);
    if constexpr (!G) if (r == 0) poll(flag + 32 * (33 + 8 * ((ks + 1 + lane % 3) & 3) + nt), tag);
    lbar();
    IT(59);
    if constexpr (!G) if (r == 0 && h == 0 && lane == 0) __hip_atomic_store(flag, tag, __ATOMIC_RELAXED, __HIP_MEMORY_SCOPE_AGENT);
    u32x4 t[6];
    const unsigned so = WS_PART + (nt * 4 + ks) * 3 * HB + wid * 2048;
#pragma unroll
    for (int z = 0; z < 3; ++z) {
      t[2 * z] = __builtin_amdgcn_raw_buffer_load_b128(rsB, lane * 16, so + z * HB, 16);
      t[2 * z + 1] = __builtin_amdgcn_raw_buffer_load_b128(rsB, lane * 16 + 1024, so + z * HB, 16);
    }
    take(3);
    k0 += t0; k1 += t1;
#pragma unroll
    for (int z = 0; z < 3; ++z) { k0 += __builtin_bit_cast(f32x4, t[2 * z]); k1 += __builtin_bit_cast(f32x4, t[2 * z + 1]); }
    const unsigned x0 = cvtpk(k0[0], k0[1]), x1 = cvtpk(k0[2], k0[3]), y0 = cvtpk(k1[0], k1[1]), y1 = cvtpk(k1[2], k1[3]);
    auto r0 = __builtin_amdgcn_permlane32_swap(x0, y0, false, false);
    auto r1 = __builtin_amdgcn_permlane32_swap(x1, y1, false, false);
    const u32x4 w4 = {r0[0], r1[0], r0[1], r1[1]};
    __builtin_amdgcn_raw_buffer_store_b128(w4, rsO, l32 * 2048 + hi * 16 + (nt * 128 + r * 32 + g * 16) * 2, 0, 0);
  }
}

__global__ __launch_bounds__(512, 2) void mha_kernel(const uint16_t* __restrict__ x, const uint16_t* __restrict__ kc,
                                                     const uint16_t* __restrict__ vc, const uint16_t* __restrict__ wq,
                                                     const uint16_t* __restrict__ wo, uint16_t* __restrict__ out, char* __restrict__ ws) {
  extern __shared__ __attribute__((aligned(16))) char smem[];
  const unsigned wid = rfl(threadIdx.x >> 6);
  const unsigned lds0 = rfl((unsigned)(uintptr_t)smem);
  if (wid >> 2) body<1>(x, kc, vc, wq, wo, out, ws, lds0, wid & 3);
  else body<0>(x, kc, vc, wq, wo, out, ws, lds0, wid & 3);
}

extern "C" void kernel_launch(void* const* d_in, const int* in_sizes, int n_in, void* d_out, int out_size, void* d_ws,
                              size_t ws_size, hipStream_t stream) {
  (void)hipFuncSetAttribute((const void*)mha_kernel, hipFuncAttributeMaxDynamicSharedMemorySize, LDSB);
  hipLaunchKernelGGL(mha_kernel, dim3(256), dim3(512), LDSB, stream, (const uint16_t*)d_in[0], (const uint16_t*)d_in[1],
                     (const uint16_t*)d_in[2], (const uint16_t*)d_in[3], (const uint16_t*)d_in[4], (uint16_t*)d_out, (char*)d_ws);
}
```

```cpp
#include <hip/hip_runtime.h>
#include <hip/hip_bf16.h>
#include <stdint.h>
#include <utility>
#include <type_traits>

typedef float f32x16 __attribute__((ext_vector_type(16)));
typedef float f32x4 __attribute__((ext_vector_type(4)));
typedef float f32x2 __attribute__((ext_vector_type(2)));
typedef __bf16 bf16x8 __attribute__((ext_vector_type(8)));
typedef __bf16 bf16x2 __attribute__((ext_vector_type(2)));
typedef short s16x4 __attribute__((ext_vector_type(4)));
typedef unsigned u32x4 __attribute__((ext_vector_type(4)));
typedef unsigned u32x2 __attribute__((ext_vector_type(2)));
typedef unsigned long long u64;
typedef __amdgpu_buffer_rsrc_t rsrc_t;
typedef __attribute__((address_space(1))) u64 gu64;

#define LAS __attribute__((address_space(3)))
#define DI __device__ __forceinline__
#define MFMA(a, b, c) __builtin_amdgcn_mfma_f32_32x32x16_bf16((a), (b), (c), 0, 0, 0)
#define SBAR() __builtin_amdgcn_sched_barrier(0)
#define PIN(x) asm volatile("" : "+v"(x))
#define BF(x) __builtin_bit_cast(bf16x8, x)
#define IC(n) std::integral_constant<int, (n)>{}

constexpr int SQ = 128, DM = 1024, NH = 32, SK = 2048, DH = 128;
constexpr int NT = SK / 64;
constexpr unsigned HB = 16384;
constexpr int LDSB = 10 * HB;
constexpr float CEXP = 0.12751743082459868f;
constexpr float SUMLIM = 1024.0f;
constexpr unsigned WS_BATCH = 4u << 20, WS_FLAG = 1u << 20, WS_PART = WS_FLAG + 65536, WS_IDLE = 3u << 20;
constexpr unsigned SPIN_CAP = 1u << 18;

#define XPOL 0
#define WPOL 0
#define OPOL 0
#define HK0 12
#define HK1 12
#define HK2 13
#define HV0 13
#define HV1 14
#define TMASK 32
#define TSTEP 0
#define PF0 2
#define PFD 2
#define PFPOL ""
#define PACE 112
#define PACE0 0
#define TOUCH 1

DI unsigned rfl(unsigned x) { return __builtin_amdgcn_readfirstlane(x); }
DI rsrc_t mkrsrc(const void* p, unsigned bytes) { return __builtin_amdgcn_make_buffer_rsrc((void*)p, (short)0, (int)bytes, 0x00020000); }
template <int N> DI void wait_bar() { asm volatile("s_waitcnt vmcnt(%0) lgkmcnt(0)\n\ts_barrier" ::"i"(N) : "memory"); }
DI void lbar() { asm volatile("s_waitcnt lgkmcnt(0)\n\ts_barrier" ::: "memory"); }
DI bf16x8 lds_b128(unsigned addr) { return *(const LAS bf16x8*)(uintptr_t)addr; }
DI f32x4 lds_f4(unsigned addr) { return *(const LAS f32x4*)(uintptr_t)addr; }
DI void sts_f4(unsigned addr, f32x4 v) { *(LAS f32x4*)(uintptr_t)addr = v; }
DI s16x4 lds_tr(unsigned addr) { return __builtin_amdgcn_ds_read_tr16_b64_v4i16((LAS s16x4*)(uintptr_t)addr); }
DI bf16x8 cat(s16x4 a, s16x4 b) { return __builtin_bit_cast(bf16x8, __builtin_shufflevector(a, b, 0, 1, 2, 3, 4, 5, 6, 7)); }
DI unsigned cvtpk(float lo, float hi) {
  f32x2 v = {lo, hi};
  return __builtin_bit_cast(unsigned, __builtin_convertvector(v, bf16x2));
}
template <int S> DI u32x4 pack8(const f32x16& v) {
  return (u32x4){cvtpk(v[8 * S + 0], v[8 * S + 1]), cvtpk(v[8 * S + 2], v[8 * S + 3]), cvtpk(v[8 * S + 4], v[8 * S + 5]), cvtpk(v[8 * S + 6], v[8 * S + 7])};
}
template <int S> DI f32x4 quad(const f32x16& v) { return (f32x4){v[4 * S], v[4 * S + 1], v[4 * S + 2], v[4 * S + 3]}; }
DI float swap_max(float v) {
  auto rr = __builtin_amdgcn_permlane32_swap(__float_as_uint(v), __float_as_uint(v), false, false);
  return fmaxf(__uint_as_float(rr[0]), __uint_as_float(rr[1]));
}
DI float swap_sum(float v) {
  auto rr = __builtin_amdgcn_permlane32_swap(__float_as_uint(v), __float_as_uint(v), false, false);
  return __uint_as_float(rr[0]) + __uint_as_float(rr[1]);
}
#define DMA4(pol) asm volatile("s_mov_b32 m0, %0\n\ts_nop 0\n\t" \
    "buffer_load_dwordx4 %1, %5, %6 offen" pol " lds\n\t" \
    "buffer_load_dwordx4 %2, %5, %7 offen offset:1024" pol " lds\n\t" \
    "buffer_load_dwordx4 %3, %5, %8 offen offset:2048" pol " lds\n\t" \
    "buffer_load_dwordx4 %4, %5, %9 offen offset:3072" pol " lds" \
    ::"s"(dst), "v"(va), "v"(vb), "v"(vc_), "v"(vd), "s"(rs), "s"(s0), "s"(s1), "s"(s2), "s"(s3) : "memory")
template <int POL>
DI void dma4(unsigned dst, unsigned va, unsigned vb, unsigned vc_, unsigned vd, rsrc_t rs, unsigned s0, unsigned s1, unsigned s2, unsigned s3) {
  if constexpr (POL == 1) DMA4(" nt");
  else if constexpr (POL == 2) DMA4(" sc1");
  else DMA4("");
}
template <class F, int... Is> DI void sfor_impl(F&& f, std::integer_sequence<int, Is...>) { (f(std::integral_constant<int, Is>{}), ...); }
template <int N, class F> DI void sfor(F&& f) { sfor_impl(f, std::make_integer_sequence<int, N>{}); }
DI unsigned realtime() { u64 t; asm volatile("s_memrealtime %0\n\ts_waitcnt lgkmcnt(0)" : "=s"(t)::"memory"); return (unsigned)t; }
DI void pace(unsigned due) {
  asm volatile("1:\n\ts_memrealtime vcc\n\ts_waitcnt lgkmcnt(0)\n\ts_sub_u32 vcc_lo, vcc_lo, %0\n\ts_cmp_lt_i32 vcc_lo, 0\n\ts_cbranch_scc1 1b" :: "s"(due) : "memory", "scc", "vcc");
}
DI void poll(const gu64* p, u64 tag) {
#pragma unroll 1
  for (unsigned n = 0; n < SPIN_CAP; ++n) {
    const u64 v = __hip_atomic_load(p, __ATOMIC_RELAXED, __HIP_MEMORY_SCOPE_AGENT);
    if (__all(v == tag)) break;
    __builtin_amdgcn_s_sleep(2);
  }
}

constexpr int hsite(int G, int k) { return G ? (k == 0 ? HV0 : k == 1 ? HV1 : 99) : (k == 0 ? HK0 : k == 1 ? HK1 : k == 2 ? HK2 : k == 3 ? 16 : 99); }
constexpr int pfsite(int it) { return PF0 >= 0 && it >= PF0 && (it - PF0) % PFD == 0 && (it - PF0) / PFD < 4 ? (it - PF0) / PFD : -1; }
constexpr int hyoung(int G, int id, int upto) {
  int seq[64] = {}, cnt[64] = {}, n = 0;
  seq[n] = 0; cnt[n++] = 4; seq[n] = 1; cnt[n++] = 4;
  seq[n] = 90; cnt[n++] = G ? (TOUCH ? 1 : 0) : (TOUCH ? 2 : 1);
  for (int it = -1; it < upto; ++it) {
    if (it + 3 <= 15) { seq[n] = it + 3; cnt[n++] = 4; }
    if (pfsite(it) >= 0) { seq[n] = 91; cnt[n++] = 1; }
    for (int k = 0; k < 4; ++k) if (hsite(G, k) == it) { seq[n] = 16 + k; cnt[n++] = 4; }
  }
  int y = 0;
  for (int i = n - 1; i >= 0; --i) { if (seq[i] == id) return y; y += cnt[i]; }
  return -1;
}
static_assert(HK0 >= 1 && HK0 <= HK1 && HK1 <= HK2 && HK2 >= 13 && HK2 <= 14 && HV0 <= HV1 && HV1 <= 15, "head schedule");
constexpr unsigned qblock(int G, int i) { return ((i + 2) % 3 == 0 ? (G ? 5 : 2) : (i + 2) % 3 == 1 ? 6 + G : 8 + G) * HB; }

struct QFrag { bf16x8 xb[4]; s16x4 wa[4][2], wb[4][2]; };

#define IT(i)
#define ITL(i)

template <int G>
DI void body(const uint16_t* __restrict__ x, const uint16_t* __restrict__ kc, const uint16_t* __restrict__ vc, const uint16_t* __restrict__ wq,
             const uint16_t* __restrict__ wo, uint16_t* __restrict__ out, char* __restrict__ ws, unsigned lds0, unsigned r) {
  constexpr unsigned g = G;
  const unsigned lane = threadIdx.x & 63, wid = 4 * g + r;
  const unsigned l32 = lane & 31, hi = lane >> 5;
  const unsigned bid = blockIdx.x, xcd = bid & 7, idx = bid >> 3;
  const unsigned h = 4 * xcd + (idx & 3), b = idx >> 2;
  IT(0);

  const unsigned l4 = lane >> 4;
  const size_t kvoff = (size_t)(b * NH + h) * SK * DH;
  const rsrc_t rsKV = mkrsrc((g ? vc : kc) + kvoff, SK * DH * 2);
  const rsrc_t rsQ = g ? mkrsrc(wq + h * DH, DM * NH * DH * 2 - h * DH * 2) : mkrsrc(x + (size_t)b * SQ * DM, SQ * DM * 2);
  const unsigned voffA = (16 * r + l4) * 256 + 16 * ((lane & 15) ^ (g ? (l4 << 2) : (l4 | ((r & 1) << 3))));
  const unsigned voffB = g ? voffA : voffA ^ 64;
  const unsigned voffQ = g ? (16 * r + l4) * 8192 + 16 * ((lane & 15) ^ (l4 << 2))
                           : (32 * r + (lane >> 3)) * 2048 + 16 * ((lane & 7) ^ l4);
  const unsigned voffQ1 = g ? voffQ : voffQ ^ 64;
  constexpr unsigned qstS = g ? 64 * 8192 : 128, qstJ = (g ? 4 * 8192 : 8 * 2048) - 1024;
  const unsigned dstw = lds0 + r * 4096;
  const unsigned kstart = (2 * b + 4 * (h & 3)) & 15;
  auto issueQ = [&](unsigned i, unsigned blk_) {
    const unsigned so = ((i + kstart) & 15) * qstS;
    dma4<g ? WPOL : XPOL>(dstw + blk_, voffQ, voffQ1, voffQ, voffQ1, rsQ, so, so + qstJ, so + 2 * qstJ, so + 3 * qstJ);
  };
  auto issueKV = [&](unsigned so, int t3) {
    dma4<1>(dstw + (3 * g + t3) * HB, voffA, voffA, voffB, voffB, rsKV, so, so, so, so);
  };

  asm volatile("s_nop 4" ::: "memory");
  issueQ(0, qblock(G, 0)); issueQ(1, qblock(G, 1));

  const unsigned ks = h >> 3, nt = h & 7, oq = (ks + nt) & 3;
  char* const wsb = ws + (size_t)b * WS_BATCH;
  const rsrc_t rsB = mkrsrc(wsb, WS_BATCH);
  const rsrc_t rsO = mkrsrc(out + (size_t)(b * SQ + 32 * oq) * DM, 32 * DM * 2);
  if constexpr (!G) {
    const unsigned ve = lane < 2 ? WS_FLAG + 4 * lane : WS_IDLE;
    asm volatile("s_mov_b32 m0, %0\n\ts_nop 0\n\tbuffer_load_dword %1, %2, 0 offen sc1 lds" ::"s"(dstw), "v"(ve), "s"(rsB) : "memory");
    if (TOUCH) asm volatile("s_mov_b32 m0, %0\n\ts_nop 0\n\tbuffer_load_dword off, %1, 0 lds" ::"s"(dstw + 256), "s"(rsO) : "memory");
  } else if (TOUCH) {
    const rsrc_t rsT = mkrsrc(wo + (size_t)ks * 1024 * DM, 1024 * DM * 2);
    asm volatile("s_mov_b32 m0, %0\n\ts_nop 0\n\tbuffer_load_dword %1, %2, 0 offen lds" ::"s"(dstw + 3 * HB), "v"((lane & TMASK) * 32768 + r * TSTEP), "s"(rsT) : "memory");
  }

  const unsigned q4 = (lane & 15) >> 2, p4 = lane & 3, blk = (lane >> 4) & 1;
  const unsigned xrow = 32 * r + l32;
  const unsigned baseX = lds0 + 128 * xrow + 16 * (hi ^ ((xrow >> 1) & 7));
  const unsigned baseW = lds0 + 256 * (8 * hi + q4) + 16 * (4 * ((2 * g) ^ q4) + 2 * blk + (p4 & 1)) + 8 * (p4 >> 1);
  const unsigned baseK = lds0 + 8192 * g + 256 * l32 + 16 * (hi ^ ((l32 & 3) | ((l32 >> 3) << 2)));
  const unsigned baseT = lds0 + 256 * (4 * hi + q4) + 16 * (4 * q4 + 2 * blk + (p4 >> 1)) + 8 * (p4 & 1);

  f32x16 qa0 = {}, qa1 = {};
  QFrag fr[2];
  auto qiter = [&](QFrag& cur, QFrag& nxt, unsigned bx, unsigned bw, bool mm) {
    unsigned sx = baseX + bx, sw0 = baseW + bw;
    PIN(sx); PIN(sw0);
    const unsigned sw1 = sw0 ^ 64;
#pragma unroll
    for (int s = 0; s < 4; ++s) {
      nxt.xb[s] = lds_b128(sx ^ (s << 5)); nxt.wa[s][0] = lds_tr(sw0 + 4096 * s); nxt.wa[s][1] = lds_tr(sw0 + 4096 * s + 1024);
      SBAR();
      if (mm) { qa0 = MFMA(cat(cur.wa[s][0], cur.wa[s][1]), cur.xb[s], qa0); PIN(qa0); }
      SBAR();
      nxt.wb[s][0] = lds_tr(sw1 + 4096 * s); nxt.wb[s][1] = lds_tr(sw1 + 4096 * s + 1024);
      SBAR();
      if (mm) { qa1 = MFMA(cat(cur.wb[s][0], cur.wb[s][1]), cur.xb[s], qa1); PIN(qa1); }
      SBAR();
    }
  };
  auto hslot = [&](auto ic) {
    constexpr int it = decltype(ic)::value;
    if constexpr (it + 3 <= 15) issueQ(it + 3, qblock(G, it + 3));
    if constexpr (pfsite(it) >= 0) {
      const rsrc_t rsT = mkrsrc(wo + (size_t)(ks * 1024 + idx * 32 + wid * 4 + pfsite(it)) * DM + (nt >> 2) * 512, 1024);
      asm volatile("s_mov_b32 m0, %0\n\ts_nop 0\n\tbuffer_load_dwordx4 %1, %2, 0 offen" PFPOL " lds" ::"s"(dstw + (3 * g + 1) * HB), "v"(lane * 16), "s"(rsT) : "memory");
    }
    sfor<4>([&](auto kc_) {
      constexpr int k = decltype(kc_)::value;
      if constexpr (hsite(G, k) == it) issueKV(k * HB, k % 3);
    });
  };
  u64 tag = 0;
  wait_bar<hyoung(G, 0, -1)>();
  IT(1);
  qiter(fr[1], fr[0], qblock(0, 0), qblock(1, 0), false);
  hslot(IC(-1));
  sfor<15>([&](auto ic) {
    constexpr int i = decltype(ic)::value;
    wait_bar<hyoung(G, i + 1, i)>();
    ITL(i + 2);
    if constexpr (G) hslot(IC(i));
    if constexpr (!G && i == 1) {
      const u32x2 e = *(const LAS u32x2*)(uintptr_t)dstw;
      tag = (((u64)rfl(e[1]) << 32) | rfl(e[0])) + 0x9E3779B97F4A7C15ull;
      asm volatile("" : "+s"(tag));
    }
    qiter(fr[i & 1], fr[(i + 1) & 1], qblock(0, i + 1), qblock(1, i + 1), true);
    if constexpr (!G) hslot(IC(i));
  });
  bf16x8 qf[8], kf[8];
  unsigned aK[8], aV[4];
  {
    QFrag& cur = fr[1];
    const unsigned xw = lds0 + 6 * HB + wid * 4096 + lane * 16;
#pragma unroll
    for (int s = 0; s < 4; ++s) qa0 = MFMA(cat(cur.wa[s][0], cur.wa[s][1]), cur.xb[s], qa0);
    PIN(qa0);
    qa1 = MFMA(cat(cur.wb[0][0], cur.wb[0][1]), cur.xb[0], qa1);
    qa1 = MFMA(cat(cur.wb[1][0], cur.wb[1][1]), cur.xb[1], qa1); PIN(qa1);
    SBAR();
#pragma unroll
    for (int k = 0; k < 8; ++k) { aK[k] = baseK ^ (k << 5); PIN(aK[k]); }
#pragma unroll
    for (int k = 0; k < 4; ++k) { aV[k] = (baseT + 3 * HB + 8192 * g) ^ (k << 6); PIN(aV[k]); }
    qa0 *= CEXP;
    qf[4 * g + 0] = BF(pack8<0>(qa0)); PIN(qf[4 * g + 0]);
    SBAR();
    qa1 = MFMA(cat(cur.wb[2][0], cur.wb[2][1]), cur.xb[2], qa1); PIN(qa1);
    SBAR();
    qf[4 * g + 1] = BF(pack8<1>(qa0)); PIN(qf[4 * g + 1]);
    SBAR();
    qa1 = MFMA(cat(cur.wb[3][0], cur.wb[3][1]), cur.xb[3], qa1); PIN(qa1);
    SBAR();
    *(LAS bf16x8*)(uintptr_t)(xw) = qf[4 * g + 0];
    *(LAS bf16x8*)(uintptr_t)(xw + 1024) = qf[4 * g + 1];
    qa1 *= CEXP;
    qf[4 * g + 2] = BF(pack8<0>(qa1)); qf[4 * g + 3] = BF(pack8<1>(qa1));
    *(LAS bf16x8*)(uintptr_t)(xw + 2048) = qf[4 * g + 2];
    *(LAS bf16x8*)(uintptr_t)(xw + 3072) = qf[4 * g + 3];
    if constexpr (G) lbar(); else wait_bar<hyoung(0, 16, 15)>();
    IT(17);
    hslot(IC(15));
    const unsigned xr = lds0 + 6 * HB + (wid ^ 4) * 4096 + lane * 16;
#pragma unroll
    for (int k = 0; k < 4; ++k) kf[4 * g + k] = lds_b128(aK[4 * g + k]);
#pragma unroll
    for (int k = 0; k < 4; ++k) { qf[4 * (1 - g) + k] = lds_b128(xr + k * 1024); kf[4 * (1 - g) + k] = lds_b128(aK[4 * (1 - g) + k]); }
  }

  f32x16 o0, o1, o2, o3, sc, negm;
  s16x4 va[8], vb[8];
  u32x4 pa0, pa1, pb0, pb1;
  float m, l;

  auto rowmax = [&](const f32x16& s) {
    float a = fmaxf(fmaxf(s[0], s[1]), s[2]), c = fmaxf(fmaxf(s[3], s[4]), s[5]);
    a = fmaxf(fmaxf(a, s[6]), s[7]); c = fmaxf(fmaxf(c, s[8]), s[9]);
    a = fmaxf(fmaxf(a, s[10]), s[11]); c = fmaxf(fmaxf(c, s[12]), s[13]);
    a = fmaxf(fmaxf(a, s[14]), s[15]);
    return swap_max(fmaxf(a, c));
  };
  auto probs = [&](u32x4& p0, u32x4& p1) {
    float sacc = 0.f;
#pragma unroll
    for (int i = 0; i < 8; ++i) {
      const float e0 = __builtin_amdgcn_exp2f(sc[2 * i]), e1 = __builtin_amdgcn_exp2f(sc[2 * i + 1]);
      sacc += e0; sacc += e1;
      if (i < 4) p0[i] = cvtpk(e0, e1); else p1[i - 4] = cvtpk(e0, e1);
    }
    return sacc;
  };
  auto guard = [&](float sacc, u32x4& p0, u32x4& p1) {
    if (__builtin_expect(__any(!(sacc <= SUMLIM)), 0)) {
      const float dl = fmaxf(rowmax(sc), 0.f);
      const float alpha = __builtin_amdgcn_exp2f(-dl);
      m += dl;
      l *= alpha;
      o0 *= alpha; o1 *= alpha; o2 *= alpha; o3 *= alpha;
      sc -= dl; negm -= dl;
      sacc = probs(p0, p1);
      PIN(m); PIN(l); PIN(sacc); PIN(p0); PIN(p1); PIN(negm); PIN(o0); PIN(o1); PIN(o2); PIN(o3);
    }
    l += sacc;
  };
  auto phaseA = [&](unsigned vo) {
    __builtin_amdgcn_s_setprio(1);
#pragma unroll
    for (int k = 0; k < 8; ++k) {
      const unsigned a_ = aV[k & 3] + vo + 4096 * (k >> 2);
      va[k] = lds_tr(a_); vb[k] = lds_tr(a_ + 2048);
      SBAR();
      sc = MFMA(kf[k], qf[k], k == 0 ? negm : sc); PIN(sc);
      SBAR();
    }
  };
  auto phaseB = [&](const u32x4& po0, const u32x4& po1, u32x4& pn0, u32x4& pn1, auto&& rd) {
    float sacc = 0.f;
    __builtin_amdgcn_s_setprio(0);
    SBAR();
#pragma unroll
    for (int i = 0; i < 8; ++i) {
      rd(i);
      SBAR();
      const bf16x8 vf = cat(va[i], vb[i]);
      const bf16x8 pf = i < 4 ? BF(po0) : BF(po1);
      if ((i & 3) == 0) { o0 = MFMA(vf, pf, o0); PIN(o0); }
      if ((i & 3) == 1) { o1 = MFMA(vf, pf, o1); PIN(o1); }
      if ((i & 3) == 2) { o2 = MFMA(vf, pf, o2); PIN(o2); }
      if ((i & 3) == 3) { o3 = MFMA(vf, pf, o3); PIN(o3); }
      SBAR();
      const float e0 = __builtin_amdgcn_exp2f(sc[2 * i]), e1 = __builtin_amdgcn_exp2f(sc[2 * i + 1]);
      if (i == 0) sacc = e0 + e1; else { sacc += e0; sacc += e1; }
      if (i < 4) { pn0[i] = cvtpk(e0, e1); PIN(pn0); } else { pn1[i - 4] = cvtpk(e0, e1); PIN(pn1); }
      PIN(sacc);
      SBAR();
    }
    asm volatile("" ::: "memory");
    return sacc;
  };

  {
    sc = (f32x16){};
#pragma unroll
    for (int k = 0; k < 4; ++k) sc = MFMA(kf[4 * g + k], qf[4 * g + k], sc);
#pragma unroll
    for (int k = 0; k < 4; ++k) sc = MFMA(kf[4 * (1 - g) + k], qf[4 * (1 - g) + k], sc);
    PIN(sc);
    SBAR();
    o0 = (f32x16){}; o1 = (f32x16){}; o2 = (f32x16){}; o3 = (f32x16){};
    PIN(o0); PIN(o1); PIN(o2); PIN(o3);
    SBAR();
    m = rowmax(sc);
#pragma unroll
    for (int i = 0; i < 16; ++i) { negm[i] = -m; sc[i] -= m; }
    PIN(negm); PIN(sc);
    if constexpr (G) lbar(); else wait_bar<hyoung(0, 17, 16)>();
    hslot(IC(16));
#pragma unroll
    for (int k = 0; k < 8; ++k) kf[k] = lds_b128(aK[k] + HB);
    l = probs(pa0, pa1);
    PIN(pa0); PIN(pa1); PIN(l);
    wait_bar<hyoung(G, G ? 16 : 18, 17)>();
    IT(18);
  }

  const rsrc_t rsW = mkrsrc(wo + (size_t)(ks * 1024 + g * 512) * DM + nt * 128, 512 * DM * 2 - nt * 256);
  s16x4 wfa[8][4], wfb[8][4];
  auto issueW = [&](unsigned p, unsigned blk_) {
    const unsigned vw = (16 * r + l4) * 2048 + 16 * ((lane & 15) ^ (l4 << 2));
    const unsigned so = ((p + b) & 7) * (64 * 2048);
    dma4<OPOL>(dstw + blk_ * HB, vw, vw, vw, vw, rsW, so, so + 7168, so + 2 * 7168, so + 3 * 7168);
  };
  auto readW = [&](int p, int k, unsigned blk_) {
    const unsigned a_ = (baseT ^ (r << 6)) + blk_ * HB + 4096 * k;
    wfa[p][k] = lds_tr(a_); wfb[p][k] = lds_tr(a_ + 2048);
  };

  unsigned nso = (g ? 2 : 4) * HB;
  unsigned due = 0;
  if (PACE && !G) due = realtime() + PACE0;
  auto step = [&](auto jc, auto lastc) {
    constexpr int j = decltype(jc)::value;
    constexpr bool last = decltype(lastc)::value;
    constexpr bool kv = !last || G || j <= 28;
    constexpr int wpos = !last ? -1 : G ? (j == 26 ? 0 : j == 28 ? 1 : -1) : (j == 25 ? 0 : j == 27 ? 1 : j == 30 ? 2 : -1);
    constexpr int wblk = wpos == 2 ? 0 : 6 + 2 * wpos + G;
    auto issue = [&]() {
      if constexpr (kv) { issueKV(nso, (G ? j + 1 : j) % 3); nso += HB; }
      if constexpr (wpos >= 0) issueW(wpos, wblk);
    };
    u32x4& po0 = (j & 1) ? pa0 : pb0; u32x4& po1 = (j & 1) ? pa1 : pb1;
    u32x4& pn0 = (j & 1) ? pb0 : pa0; u32x4& pn1 = (j & 1) ? pb1 : pa1;
    if constexpr (G) issue();
    phaseA(((j + 2) % 3) * HB);
    const float sacc = phaseB(po0, po1, pn0, pn1, [&](int i) {
      if (i < 4) { kf[2 * i] = lds_b128(aK[2 * i] + ((j + 1) % 3) * HB); kf[2 * i + 1] = lds_b128(aK[2 * i + 1] + ((j + 1) % 3) * HB); }
    });
    if constexpr (!G) issue();
    guard(sacc, pn0, pn1);
    if constexpr (PACE && !G && j < 30) { due += PACE; pace(due); }
    wait_bar<(kv ? 4 : 0) + (wpos >= 0 ? 4 : 0)>();
  };
  for (unsigned jj = 0; jj < 4; ++jj)
    sfor<6>([&](auto c) { step(IC(decltype(c)::value + 1), std::false_type{}); ITL(19 + 6 * jj + decltype(c)::value); });
  sfor<6>([&](auto c) { step(IC(decltype(c)::value + 25), std::true_type{}); ITL(43 + decltype(c)::value); });
  IT(48);

  {
    if constexpr (G) issueW(2, 1);
    phaseA(0);
    const float sacc = phaseB(pa0, pa1, pb0, pb1, [&](int i) { readW(i >> 2, i & 3, 6 + 2 * (i >> 2) + g); });
    guard(sacc, pb0, pb1);
    l = swap_sum(l);
    const unsigned xl = lds0 + lane * 16;
    *(LAS u32x4*)(uintptr_t)(xl + 2 * HB + wid * 2048) = pb0;
    *(LAS u32x4*)(uintptr_t)(xl + 2 * HB + wid * 2048 + 1024) = pb1;
    *(LAS f32x2*)(uintptr_t)(lds0 + 5 * HB + wid * 512 + lane * 8) = (f32x2){m, l};
    lbar();
    IT(49);
    {
      const unsigned xw = xl + 6 * HB + wid * 8192;
      const f32x16 sa = g ? o0 : o2, sb = g ? o1 : o3;
      sts_f4(xw, quad<0>(sa)); sts_f4(xw + 1024, quad<1>(sa)); sts_f4(xw + 2048, quad<2>(sa)); sts_f4(xw + 3072, quad<3>(sa));
      sts_f4(xw + 4096, quad<0>(sb)); sts_f4(xw + 5120, quad<1>(sb)); sts_f4(xw + 6144, quad<2>(sb)); sts_f4(xw + 7168, quad<3>(sb));
    }
    const unsigned pw = wid ^ 4;
    const bf16x8 pp0 = lds_b128(xl + 2 * HB + pw * 2048), pp1 = lds_b128(xl + 2 * HB + pw * 2048 + 1024);
    const f32x2 ml = *(const LAS f32x2*)(uintptr_t)(lds0 + 5 * HB + pw * 512 + lane * 8);
    const float mf = fmaxf(m, ml[0]);
    const float a0 = __builtin_amdgcn_exp2f(m - mf), a1 = __builtin_amdgcn_exp2f(ml[0] - mf);
    const float inv = 1.0f / (a0 * l + a1 * ml[1]);
    float c0 = a0 * inv, c1 = a1 * inv;
    PIN(c0); PIN(c1);
    wait_bar<0>();
    IT(50);
#pragma unroll
    for (int k = 0; k < 8; ++k) {
      const unsigned a_ = aV[2 * g + (k & 1)] + HB + 4096 * ((k >> 1) & 1) + (k >> 2) * (g ? -8192 : 8192);
      va[k] = lds_tr(a_); vb[k] = lds_tr(a_ + 2048);
    }
    f32x16 ea = g ? o2 : o0, eb = g ? o3 : o1, ua, ub;
    const unsigned xr = xl + 6 * HB + pw * 8192;
#pragma unroll
    for (int i = 0; i < 4; ++i) {
      const f32x4 t0 = lds_f4(xr + i * 1024), t1 = lds_f4(xr + 4096 + i * 1024);
#pragma unroll
      for (int e = 0; e < 4; ++e) { ua[4 * i + e] = t0[e]; ub[4 * i + e] = t1[e]; }
    }
#pragma unroll
    for (int k = 0; k < 4; ++k) readW(2, k, g);
    SBAR();
    ea = MFMA(cat(va[0], vb[0]), BF(pb0), ea); eb = MFMA(cat(va[1], vb[1]), BF(pb0), eb);
    ea = MFMA(cat(va[2], vb[2]), BF(pb1), ea); eb = MFMA(cat(va[3], vb[3]), BF(pb1), eb);
    ua = MFMA(cat(va[4], vb[4]), pp0, ua); ub = MFMA(cat(va[5], vb[5]), pp0, ub);
    ua = MFMA(cat(va[6], vb[6]), pp1, ua); ub = MFMA(cat(va[7], vb[7]), pp1, ub);
    PIN(ea); PIN(eb); PIN(ua); PIN(ub);
    SBAR();
    lbar();
    issueW(3, g); issueW(4, 2 + g);
    SBAR();
    ea = ea * c0 + ua * c1;
    const unsigned vo = ((h * 4 + r) * 8 + 4 * g) * 1024 + lane * 16;
    __builtin_amdgcn_raw_buffer_store_b128(pack8<0>(ea), rsB, vo, 0, 16);
    __builtin_amdgcn_raw_buffer_store_b128(pack8<1>(ea), rsB, vo + 1024, 0, 16);
    eb = eb * c0 + ub * c1;
    __builtin_amdgcn_raw_buffer_store_b128(pack8<0>(eb), rsB, vo + 2048, 0, 16);
    __builtin_amdgcn_raw_buffer_store_b128(pack8<1>(eb), rsB, vo + 3072, 0, 16);
    IT(51);
    wait_bar<0>();
    IT(52);
  }
  gu64* const flag = (gu64*)(wsb + WS_FLAG);
  if constexpr (!G) if (r == 0 && lane == 0) __hip_atomic_store(flag + 32 * (1 + h), tag, __ATOMIC_RELAXED, __HIP_MEMORY_SCOPE_AGENT);
  issueW(5, 4 + g); issueW(6, 6 + g); issueW(7, 8 + g);
#pragma unroll
  for (int k = 0; k < 8; ++k) readW(3 + (k >> 2), k & 3, 2 * (k >> 2) + g);
  asm volatile("" ::: "memory");
  if constexpr (!G) if (r == 0) poll(flag + 32 * (1 + 8 * ks + (lane & 7)), tag);
  wait_bar<0>();
  IT(53);

  auto issueD = [&](unsigned s) {
    const unsigned c = (4 * (s & 1) + r + b) & 7, q = (oq + 1 + (s >> 1)) & 3;
    const unsigned so = (8 * ks + 4 * g + (c >> 1)) * 32768 + q * 8192 + (c & 1) * 4096;
    const unsigned vl = lane * 16;
    dma4<2>(dstw + (2 * (s & 3) + g) * HB, vl, vl, vl, vl, rsB, so, so, so, so);
  };
  issueD(0); issueD(1);
#pragma unroll
  for (int k = 0; k < 12; ++k) readW(5 + (k >> 2), k & 3, 4 + 2 * (k >> 2) + g);
  wait_bar<4>();
  IT(54);
  issueD(2); issueD(3);
  {
    unsigned aD[2] = {lds0 + g * HB + lane * 16, lds0 + (4 + g) * HB + lane * 16};
    PIN(aD[0]); PIN(aD[1]);
    unsigned aR = lds0 + 8 * HB + r * 4096 + lane * 16;
    PIN(aR);
    bf16x8 of[8];
    f32x16 acc[2];
    f32x4 k0, k1, t0, t1;
    auto rdO = [&](int f) { of[f & 7] = lds_b128(aD[(f >> 5) & 1] + ((f >> 4) & 1) * 2 * HB + (f & 15) * 1024); };
    auto give = [&](int z) {
      const f32x16& a = acc[z & 1];
      sts_f4(aR + (z & 1) * HB + g * 2048, quad<2 - 2 * G>(a)); sts_f4(aR + (z & 1) * HB + g * 2048 + 1024, quad<3 - 2 * G>(a));
      k0 = quad<2 * G>(a); k1 = quad<2 * G + 1>(a);
    };
    auto take = [&](int z) {
      t0 = lds_f4(aR + (z & 1) * HB + (1 - g) * 2048); t1 = lds_f4(aR + (z & 1) * HB + (1 - g) * 2048 + 1024);
    };
    auto send = [&](int z) {
      const unsigned vo = WS_PART + ((nt * 4 + ((ks + 1 + z) & 3)) * 3 + z) * HB + wid * 2048 + lane * 16;
      k0 += t0; k1 += t1;
      __builtin_amdgcn_raw_buffer_store_b128(__builtin_bit_cast(u32x4, k0), rsB, vo, 0, 16);
      __builtin_amdgcn_raw_buffer_store_b128(__builtin_bit_cast(u32x4, k1), rsB, vo + 1024, 0, 16);
      asm volatile("s_nop 1" ::: "memory");
    };
#pragma unroll
    for (int f = 0; f < 6; ++f) rdO(f);
    sfor<8>([&](auto sc_) {
      constexpr int s = decltype(sc_)::value, z = s >> 1;
      sfor<16>([&](auto ic) {
        constexpr int i = decltype(ic)::value, f = 16 * s + i;
        if constexpr (i <= 9) rdO(f + 6);
        if constexpr (i == 2 && (s == 2 || s == 4 || s == 6)) give(z - 1);
        if constexpr (i == 4 && s == 6) { wait_bar<0>(); take(2); }
        if constexpr (i == 6 && s == 6) send(2);
        if constexpr (i == 8 && s == 7) {
          wait_bar<0>();
          if constexpr (!G) if (r == 0 && lane == 0) __hip_atomic_store(flag + 32 * (33 + h), tag, __ATOMIC_RELAXED, __HIP_MEMORY_SCOPE_AGENT);
        }
        if constexpr (i == 13 && s < 7) {
          if constexpr (s < 6) wait_bar<(s < 5 ? 8 : 4)>();
          if constexpr (s < 4) issueD(s + 4);
          rdO(f + 3); rdO(f + 4); rdO(f + 5);
          if constexpr (s == 2 || s == 4) take(z - 1);
        }
        if constexpr (i == 14 && s < 7) { rdO(f + 5); rdO(f + 6); }
        if constexpr (i == 15 && s < 7) { rdO(f + 6); if constexpr (s == 2 || s == 4) send(z - 1); }
        SBAR();
        constexpr int p = 4 * (s & 1) + (i >> 2), k = i & 3;
        if constexpr (i == 0 && !(s & 1)) acc[z & 1] = MFMA(cat(wfa[p][k], wfb[p][k]), of[f & 7], (f32x16){});
        else acc[z & 1] = MFMA(cat(wfa[p][k], wfb[p][k]), of[f & 7], acc[z & 1]);
        PIN(acc[z & 1]);
        SBAR();
      });
      if constexpr (s & 1) IT(55 + z);
    });
    give(3);
    if constexpr (!G) if (r == 0) poll(flag + 32 * (33 + 8 * ((ks + 1 + lane % 3) & 3) + nt), tag);
    lbar();
    IT(59);
    if constexpr (!G) if (r == 0 && h == 0 && lane == 0) __hip_atomic_store(flag, tag, __ATOMIC_RELAXED, __HIP_MEMORY_SCOPE_AGENT);
    u32x4 t[6];
    const unsigned so = WS_PART + (nt * 4 + ks) * 3 * HB + wid * 2048;
#pragma unroll
    for (int z = 0; z < 3; ++z) {
      t[2 * z] = __builtin_amdgcn_raw_buffer_load_b128(rsB, lane * 16, so + z * HB, 16);
      t[2 * z + 1] = __builtin_amdgcn_raw_buffer_load_b128(rsB, lane * 16 + 1024, so + z * HB, 16);
    }
    take(3);
    k0 += t0; k1 += t1;
#pragma unroll
    for (int z = 0; z < 3; ++z) { k0 += __builtin_bit_cast(f32x4, t[2 * z]); k1 += __builtin_bit_cast(f32x4, t[2 * z + 1]); }
    const unsigned x0 = cvtpk(k0[0], k0[1]), x1 = cvtpk(k0[2], k0[3]), y0 = cvtpk(k1[0], k1[1]), y1 = cvtpk(k1[2], k1[3]);
    auto r0 = __builtin_amdgcn_permlane32_swap(x0, y0, false, false);
    auto r1 = __builtin_amdgcn_permlane32_swap(x1, y1, false, false);
    const u32x4 w4 = {r0[0], r1[0], r0[1], r1[1]};
    __builtin_amdgcn_raw_buffer_store_b128(w4, rsO, l32 * 2048 + hi * 16 + (nt * 128 + r * 32 + g * 16) * 2, 0, 0);
  }
}

__global__ __launch_bounds__(512, 2) void mha_kernel(const uint16_t* __restrict__ x, const uint16_t* __restrict__ kc,
                                                     const uint16_t* __restrict__ vc, const uint16_t* __restrict__ wq,
                                                     const uint16_t* __restrict__ wo, uint16_t* __restrict__ out, char* __restrict__ ws) {
  extern __shared__ __attribute__((aligned(16))) char smem[];
  const unsigned wid = rfl(threadIdx.x >> 6);
  const unsigned lds0 = rfl((unsigned)(uintptr_t)smem);
  if (wid >> 2) body<1>(x, kc, vc, wq, wo, out, ws, lds0, wid & 3);
  else body<0>(x, kc, vc, wq, wo, out, ws, lds0, wid & 3);
}

extern "C" void kernel_launch(void* const* d_in, const int* in_sizes, int n_in, void* d_out, int out_size, void* d_ws,
                              size_t ws_size, hipStream_t stream) {
  (void)hipFuncSetAttribute((const void*)mha_kernel, hipFuncAttributeMaxDynamicSharedMemorySize, LDSB);
  hipLaunchKernelGGL(mha_kernel, dim3(256), dim3(512), LDSB, stream, (const uint16_t*)d_in[0], (const uint16_t*)d_in[1],
                     (const uint16_t*)d_in[2], (const uint16_t*)d_in[3], (const uint16_t*)d_in[4], (uint16_t*)d_out, (char*)d_ws);
}
```

```cpp
#include <hip/hip_runtime.h>
#include <hip/hip_bf16.h>
#include <stdint.h>
#include <utility>
#include <type_traits>

typedef float f32x16 __attribute__((ext_vector_type(16)));
typedef float f32x4 __attribute__((ext_vector_type(4)));
typedef float f32x2 __attribute__((ext_vector_type(2)));
typedef __bf16 bf16x8 __attribute__((ext_vector_type(8)));
typedef __bf16 bf16x2 __attribute__((ext_vector_type(2)));
typedef short s16x4 __attribute__((ext_vector_type(4)));
typedef unsigned u32x4 __attribute__((ext_vector_type(4)));
typedef unsigned u32x2 __attribute__((ext_vector_type(2)));
typedef unsigned long long u64;
typedef __amdgpu_buffer_rsrc_t rsrc_t;
typedef __attribute__((address_space(1))) u64 gu64;

#define LAS __attribute__((address_space(3)))
#define DI __device__ __forceinline__
#define MFMA(a, b, c) __builtin_amdgcn_mfma_f32_32x32x16_bf16((a), (b), (c), 0, 0, 0)
#define SBAR() __builtin_amdgcn_sched_barrier(0)
#define PIN(x) asm volatile("" : "+v"(x))
#define BF(x) __builtin_bit_cast(bf16x8, x)
#define IC(n) std::integral_constant<int, (n)>{}

constexpr int SQ = 128, DM = 1024, NH = 32, SK = 2048, DH = 128;
constexpr int NT = SK / 64;
constexpr unsigned HB = 16384;
constexpr int LDSB = 10 * HB;
constexpr float CEXP = 0.12751743082459868f;
constexpr float SUMLIM = 1024.0f;
constexpr unsigned WS_BATCH = 4u << 20, WS_FLAG = 1u << 20, WS_PART = WS_FLAG + 65536, WS_IDLE = 3u << 20;
constexpr unsigned SPIN_CAP = 1u << 18;

#define XPOL 0
#define WPOL 0
#define OPOL 0
#define HK0 12
#define HK1 12
#define HK2 13
#define HV0 13
#define HV1 14
#define TMASK 32
#define TSTEP 0
#define PF0 2
#define PFD 2
#define PFPOL ""
#define PACE 112
#define PEND 27
#define DST 6
#define PACE0 0
#define TOUCH 1

DI unsigned rfl(unsigned x) { return __builtin_amdgcn_readfirstlane(x); }
DI rsrc_t mkrsrc(const void* p, unsigned bytes) { return __builtin_amdgcn_make_buffer_rsrc((void*)p, (short)0, (int)bytes, 0x00020000); }
template <int N> DI void wait_bar() { asm volatile("s_waitcnt vmcnt(%0) lgkmcnt(0)\n\ts_barrier" ::"i"(N) : "memory"); }
DI void lbar() { asm volatile("s_waitcnt lgkmcnt(0)\n\ts_barrier" ::: "memory"); }
DI bf16x8 lds_b128(unsigned addr) { return *(const LAS bf16x8*)(uintptr_t)addr; }
DI f32x4 lds_f4(unsigned addr) { return *(const LAS f32x4*)(uintptr_t)addr; }
DI void sts_f4(unsigned addr, f32x4 v) { *(LAS f32x4*)(uintptr_t)addr = v; }
DI s16x4 lds_tr(unsigned addr) { return __builtin_amdgcn_ds_read_tr16_b64_v4i16((LAS s16x4*)(uintptr_t)addr); }
DI bf16x8 cat(s16x4 a, s16x4 b) { return __builtin_bit_cast(bf16x8, __builtin_shufflevector(a, b, 0, 1, 2, 3, 4, 5, 6, 7)); }
DI unsigned cvtpk(float lo, float hi) {
  f32x2 v = {lo, hi};
  return __builtin_bit_cast(unsigned, __builtin_convertvector(v, bf16x2));
}
template <int S> DI u32x4 pack8(const f32x16& v) {
  return (u32x4){cvtpk(v[8 * S + 0], v[8 * S + 1]), cvtpk(v[8 * S + 2], v[8 * S + 3]), cvtpk(v[8 * S + 4], v[8 * S + 5]), cvtpk(v[8 * S + 6], v[8 * S + 7])};
}
template <int S> DI f32x4 quad(const f32x16& v) { return (f32x4){v[4 * S], v[4 * S + 1], v[4 * S + 2], v[4 * S + 3]}; }
DI float swap_max(float v) {
  auto rr = __builtin_amdgcn_permlane32_swap(__float_as_uint(v), __float_as_uint(v), false, false);
  return fmaxf(__uint_as_float(rr[0]), __uint_as_float(rr[1]));
}
DI float swap_sum(float v) {
  auto rr = __builtin_amdgcn_permlane32_swap(__float_as_uint(v), __float_as_uint(v), false, false);
  return __uint_as_float(rr[0]) + __uint_as_float(rr[1]);
}
#define DMA4(pol) asm volatile("s_mov_b32 m0, %0\n\ts_nop 0\n\t" \
    "buffer_load_dwordx4 %1, %5, %6 offen" pol " lds\n\t" \
    "buffer_load_dwordx4 %2, %5, %7 offen offset:1024" pol " lds\n\t" \
    "buffer_load_dwordx4 %3, %5, %8 offen offset:2048" pol " lds\n\t" \
    "buffer_load_dwordx4 %4, %5, %9 offen offset:3072" pol " lds" \
    ::"s"(dst), "v"(va), "v"(vb), "v"(vc_), "v"(vd), "s"(rs), "s"(s0), "s"(s1), "s"(s2), "s"(s3) : "memory")
template <int POL>
DI void dma4(unsigned dst, unsigned va, unsigned vb, unsigned vc_, unsigned vd, rsrc_t rs, unsigned s0, unsigned s1, unsigned s2, unsigned s3) {
  if constexpr (POL == 1) DMA4(" nt");
  else if constexpr (POL == 2) DMA4(" sc1");
  else DMA4("");
}
template <class F, int... Is> DI void sfor_impl(F&& f, std::integer_sequence<int, Is...>) { (f(std::integral_constant<int, Is>{}), ...); }
template <int N, class F> DI void sfor(F&& f) { sfor_impl(f, std::make_integer_sequence<int, N>{}); }
DI unsigned realtime() { u64 t; asm volatile("s_memrealtime %0\n\ts_waitcnt lgkmcnt(0)" : "=s"(t)::"memory"); return (unsigned)t; }
DI void pace(unsigned due) {
  asm volatile("1:\n\ts_memrealtime vcc\n\ts_waitcnt lgkmcnt(0)\n\ts_sub_u32 vcc_lo, vcc_lo, %0\n\ts_cmp_lt_i32 vcc_lo, 0\n\ts_cbranch_scc1 1b" :: "s"(due) : "memory", "scc", "vcc");
}
DI void poll(const gu64* p, u64 tag) {
#pragma unroll 1
  for (unsigned n = 0; n < SPIN_CAP; ++n) {
    const u64 v = __hip_atomic_load(p, __ATOMIC_RELAXED, __HIP_MEMORY_SCOPE_AGENT);
    if (__all(v == tag)) break;
    __builtin_amdgcn_s_sleep(2);
  }
}

constexpr int hsite(int G, int k) { return G ? (k == 0 ? HV0 : k == 1 ? HV1 : 99) : (k == 0 ? HK0 : k == 1 ? HK1 : k == 2 ? HK2 : k == 3 ? 16 : 99); }
constexpr int pfsite(int it) { return PF0 >= 0 && it >= PF0 && (it - PF0) % PFD == 0 && (it - PF0) / PFD < 4 ? (it - PF0) / PFD : -1; }
constexpr int hyoung(int G, int id, int upto) {
  int seq[64] = {}, cnt[64] = {}, n = 0;
  seq[n] = 0; cnt[n++] = 4; seq[n] = 1; cnt[n++] = 4;
  seq[n] = 90; cnt[n++] = G ? (TOUCH ? 1 : 0) : (TOUCH ? 2 : 1);
  for (int it = -1; it < upto; ++it) {
    if (it + 3 <= 15) { seq[n] = it + 3; cnt[n++] = 4; }
    if (pfsite(it) >= 0) { seq[n] = 91; cnt[n++] = 1; }
    for (int k = 0; k < 4; ++k) if (hsite(G, k) == it) { seq[n] = 16 + k; cnt[n++] = 4; }
  }
  int y = 0;
  for (int i = n - 1; i >= 0; --i) { if (seq[i] == id) return y; y += cnt[i]; }
  return -1;
}
static_assert(HK0 >= 1 && HK0 <= HK1 && HK1 <= HK2 && HK2 >= 13 && HK2 <= 14 && HV0 <= HV1 && HV1 <= 15, "head schedule");
constexpr unsigned qblock(int G, int i) { return ((i + 2) % 3 == 0 ? (G ? 5 : 2) : (i + 2) % 3 == 1 ? 6 + G : 8 + G) * HB; }

struct QFrag { bf16x8 xb[4]; s16x4 wa[4][2], wb[4][2]; };

#define IT(i)
#define ITL(i)

template <int G>
DI void body(const uint16_t* __restrict__ x, const uint16_t* __restrict__ kc, const uint16_t* __restrict__ vc, const uint16_t* __restrict__ wq,
             const uint16_t* __restrict__ wo, uint16_t* __restrict__ out, char* __restrict__ ws, unsigned lds0, unsigned r) {
  constexpr unsigned g = G;
  const unsigned lane = threadIdx.x & 63, wid = 4 * g + r;
  const unsigned l32 = lane & 31, hi = lane >> 5;
  const unsigned bid = blockIdx.x, xcd = bid & 7, idx = bid >> 3;
  const unsigned h = 4 * xcd + (idx & 3), b = idx >> 2;
  IT(0);

  const unsigned l4 = lane >> 4;
  const size_t kvoff = (size_t)(b * NH + h) * SK * DH;
  const rsrc_t rsKV = mkrsrc((g ? vc : kc) + kvoff, SK * DH * 2);
  const rsrc_t rsQ = g ? mkrsrc(wq + h * DH, DM * NH * DH * 2 - h * DH * 2) : mkrsrc(x + (size_t)b * SQ * DM, SQ * DM * 2);
  const unsigned voffA = (16 * r + l4) * 256 + 16 * ((lane & 15) ^ (g ? (l4 << 2) : (l4 | ((r & 1) << 3))));
  const unsigned voffB = g ? voffA : voffA ^ 64;
  const unsigned voffQ = g ? (16 * r + l4) * 8192 + 16 * ((lane & 15) ^ (l4 << 2))
                           : (32 * r + (lane >> 3)) * 2048 + 16 * ((lane & 7) ^ l4);
  const unsigned voffQ1 = g ? voffQ : voffQ ^ 64;
  constexpr unsigned qstS = g ? 64 * 8192 : 128, qstJ = (g ? 4 * 8192 : 8 * 2048) - 1024;
  const unsigned dstw = lds0 + r * 4096;
  const unsigned kstart = (2 * b + 4 * (h & 3)) & 15;
  auto issueQ = [&](unsigned i, unsigned blk_) {
    const unsigned so = ((i + kstart) & 15) * qstS;
    dma4<g ? WPOL : XPOL>(dstw + blk_, voffQ, voffQ1, voffQ, voffQ1, rsQ, so, so + qstJ, so + 2 * qstJ, so + 3 * qstJ);
  };
  auto issueKV = [&](unsigned so, int t3) {
    dma4<1>(dstw + (3 * g + t3) * HB, voffA, voffA, voffB, voffB, rsKV, so, so, so, so);
  };

  asm volatile("s_nop 4" ::: "memory");
  issueQ(0, qblock(G, 0)); issueQ(1, qblock(G, 1));

  const unsigned ks = h >> 3, nt = h & 7, oq = (ks + nt) & 3;
  char* const wsb = ws + (size_t)b * WS_BATCH;
  const rsrc_t rsB = mkrsrc(wsb, WS_BATCH);
  const rsrc_t rsO = mkrsrc(out + (size_t)(b * SQ + 32 * oq) * DM, 32 * DM * 2);
  if constexpr (!G) {
    const unsigned ve = lane < 2 ? WS_FLAG + 4 * lane : WS_IDLE;
    asm volatile("s_mov_b32 m0, %0\n\ts_nop 0\n\tbuffer_load_dword %1, %2, 0 offen sc1 lds" ::"s"(dstw), "v"(ve), "s"(rsB) : "memory");
    if (TOUCH) asm volatile("s_mov_b32 m0, %0\n\ts_nop 0\n\tbuffer_load_dword off, %1, 0 lds" ::"s"(dstw + 256), "s"(rsO) : "memory");
  } else if (TOUCH) {
    const rsrc_t rsT = mkrsrc(wo + (size_t)ks * 1024 * DM, 1024 * DM * 2);
    asm volatile("s_mov_b32 m0, %0\n\ts_nop 0\n\tbuffer_load_dword %1, %2, 0 offen lds" ::"s"(dstw + 3 * HB), "v"((lane & TMASK) * 32768 + r * TSTEP), "s"(rsT) : "memory");
  }

  const unsigned q4 = (lane & 15) >> 2, p4 = lane & 3, blk = (lane >> 4) & 1;
  const unsigned xrow = 32 * r + l32;
  const unsigned baseX = lds0 + 128 * xrow + 16 * (hi ^ ((xrow >> 1) & 7));
  const unsigned baseW = lds0 + 256 * (8 * hi + q4) + 16 * (4 * ((2 * g) ^ q4) + 2 * blk + (p4 & 1)) + 8 * (p4 >> 1);
  const unsigned baseK = lds0 + 8192 * g + 256 * l32 + 16 * (hi ^ ((l32 & 3) | ((l32 >> 3) << 2)));
  const unsigned baseT = lds0 + 256 * (4 * hi + q4) + 16 * (4 * q4 + 2 * blk + (p4 >> 1)) + 8 * (p4 & 1);

  f32x16 qa0 = {}, qa1 = {};
  QFrag fr[2];
  auto qiter = [&](QFrag& cur, QFrag& nxt, unsigned bx, unsigned bw, bool mm) {
    unsigned sx = baseX + bx, sw0 = baseW + bw;
    PIN(sx); PIN(sw0);
    const unsigned sw1 = sw0 ^ 64;
#pragma unroll
    for (int s = 0; s < 4; ++s) {
      nxt.xb[s] = lds_b128(sx ^ (s << 5)); nxt.wa[s][0] = lds_tr(sw0 + 4096 * s); nxt.wa[s][1] = lds_tr(sw0 + 4096 * s + 1024);
      SBAR();
      if (mm) { qa0 = MFMA(cat(cur.wa[s][0], cur.wa[s][1]), cur.xb[s], qa0); PIN(qa0); }
      SBAR();
      nxt.wb[s][0] = lds_tr(sw1 + 4096 * s); nxt.wb[s][1] = lds_tr(sw1 + 4096 * s + 1024);
      SBAR();
      if (mm) { qa1 = MFMA(cat(cur.wb[s][0], cur.wb[s][1]), cur.xb[s], qa1); PIN(qa1); }
      SBAR();
    }
  };
  auto hslot = [&](auto ic) {
    constexpr int it = decltype(ic)::value;
    if constexpr (it + 3 <= 15) issueQ(it + 3, qblock(G, it + 3));
    if constexpr (pfsite(it) >= 0) {
      const rsrc_t rsT = mkrsrc(wo + (size_t)(ks * 1024 + idx * 32 + wid * 4 + pfsite(it)) * DM + (nt >> 2) * 512, 1024);
      asm volatile("s_mov_b32 m0, %0\n\ts_nop 0\n\tbuffer_load_dwordx4 %1, %2, 0 offen" PFPOL " lds" ::"s"(dstw + (3 * g + 1) * HB), "v"(lane * 16), "s"(rsT) : "memory");
    }
    sfor<4>([&](auto kc_) {
      constexpr int k = decltype(kc_)::value;
      if constexpr (hsite(G, k) == it) issueKV(k * HB, k % 3);
    });
  };
  u64 tag = 0;
  wait_bar<hyoung(G, 0, -1)>();
  IT(1);
  qiter(fr[1], fr[0], qblock(0, 0), qblock(1, 0), false);
  hslot(IC(-1));
  sfor<15>([&](auto ic) {
    constexpr int i = decltype(ic)::value;
    wait_bar<hyoung(G, i + 1, i)>();
    ITL(i + 2);
    if constexpr (G) hslot(IC(i));
    if constexpr (!G && i == 1) {
      const u32x2 e = *(const LAS u32x2*)(uintptr_t)dstw;
      tag = (((u64)rfl(e[1]) << 32) | rfl(e[0])) + 0x9E3779B97F4A7C15ull;
      asm volatile("" : "+s"(tag));
    }
    qiter(fr[i & 1], fr[(i + 1) & 1], qblock(0, i + 1), qblock(1, i + 1), true);
    if constexpr (!G) hslot(IC(i));
  });
  bf16x8 qf[8], kf[8];
  unsigned aK[8], aV[4];
  {
    QFrag& cur = fr[1];
    const unsigned xw = lds0 + 6 * HB + wid * 4096 + lane * 16;
#pragma unroll
    for (int s = 0; s < 4; ++s) qa0 = MFMA(cat(cur.wa[s][0], cur.wa[s][1]), cur.xb[s], qa0);
    PIN(qa0);
    qa1 = MFMA(cat(cur.wb[0][0], cur.wb[0][1]), cur.xb[0], qa1);
    qa1 = MFMA(cat(cur.wb[1][0], cur.wb[1][1]), cur.xb[1], qa1); PIN(qa1);
    SBAR();
#pragma unroll
    for (int k = 0; k < 8; ++k) { aK[k] = baseK ^ (k << 5); PIN(aK[k]); }
#pragma unroll
    for (int k = 0; k < 4; ++k) { aV[k] = (baseT + 3 * HB + 8192 * g) ^ (k << 6); PIN(aV[k]); }
    qa0 *= CEXP;
    qf[4 * g + 0] = BF(pack8<0>(qa0)); PIN(qf[4 * g + 0]);
    SBAR();
    qa1 = MFMA(cat(cur.wb[2][0], cur.wb[2][1]), cur.xb[2], qa1); PIN(qa1);
    SBAR();
    qf[4 * g + 1] = BF(pack8<1>(qa0)); PIN(qf[4 * g + 1]);
    SBAR();
    qa1 = MFMA(cat(cur.wb[3][0], cur.wb[3][1]), cur.xb[3], qa1); PIN(qa1);
    SBAR();
    *(LAS bf16x8*)(uintptr_t)(xw) = qf[4 * g + 0];
    *(LAS bf16x8*)(uintptr_t)(xw + 1024) = qf[4 * g + 1];
    qa1 *= CEXP;
    qf[4 * g + 2] = BF(pack8<0>(qa1)); qf[4 * g + 3] = BF(pack8<1>(qa1));
    *(LAS bf16x8*)(uintptr_t)(xw + 2048) = qf[4 * g + 2];
    *(LAS bf16x8*)(uintptr_t)(xw + 3072) = qf[4 * g + 3];
    if constexpr (G) lbar(); else wait_bar<hyoung(0, 16, 15)>();
    IT(17);
    hslot(IC(15));
    const unsigned xr = lds0 + 6 * HB + (wid ^ 4) * 4096 + lane * 16;
#pragma unroll
    for (int k = 0; k < 4; ++k) kf[4 * g + k] = lds_b128(aK[4 * g + k]);
#pragma unroll
    for (int k = 0; k < 4; ++k) { qf[4 * (1 - g) + k] = lds_b128(xr + k * 1024); kf[4 * (1 - g) + k] = lds_b128(aK[4 * (1 - g) + k]); }
  }

  f32x16 o0, o1, o2, o3, sc, negm;
  s16x4 va[8], vb[8];
  u32x4 pa0, pa1, pb0, pb1;
  float m, l;

  auto rowmax = [&](const f32x16& s) {
    float a = fmaxf(fmaxf(s[0], s[1]), s[2]), c = fmaxf(fmaxf(s[3], s[4]), s[5]);
    a = fmaxf(fmaxf(a, s[6]), s[7]); c = fmaxf(fmaxf(c, s[8]), s[9]);
    a = fmaxf(fmaxf(a, s[10]), s[11]); c = fmaxf(fmaxf(c, s[12]), s[13]);
    a = fmaxf(fmaxf(a, s[14]), s[15]);
    return swap_max(fmaxf(a, c));
  };
  auto probs = [&](u32x4& p0, u32x4& p1) {
    float sacc = 0.f;
#pragma unroll
    for (int i = 0; i < 8; ++i) {
      const float e0 = __builtin_amdgcn_exp2f(sc[2 * i]), e1 = __builtin_amdgcn_exp2f(sc[2 * i + 1]);
      sacc += e0; sacc += e1;
      if (i < 4) p0[i] = cvtpk(e0, e1); else p1[i - 4] = cvtpk(e0, e1);
    }
    return sacc;
  };
  auto guard = [&](float sacc, u32x4& p0, u32x4& p1) {
    if (__builtin_expect(__any(!(sacc <= SUMLIM)), 0)) {
      const float dl = fmaxf(rowmax(sc), 0.f);
      const float alpha = __builtin_amdgcn_exp2f(-dl);
      m += dl;
      l *= alpha;
      o0 *= alpha; o1 *= alpha; o2 *= alpha; o3 *= alpha;
      sc -= dl; negm -= dl;
      sacc = probs(p0, p1);
      PIN(m); PIN(l); PIN(sacc); PIN(p0); PIN(p1); PIN(negm); PIN(o0); PIN(o1); PIN(o2); PIN(o3);
    }
    l += sacc;
  };
  auto phaseA = [&](unsigned vo) {
    __builtin_amdgcn_s_setprio(1);
#pragma unroll
    for (int k = 0; k < 8; ++k) {
      const unsigned a_ = aV[k & 3] + vo + 4096 * (k >> 2);
      va[k] = lds_tr(a_); vb[k] = lds_tr(a_ + 2048);
      SBAR();
      sc = MFMA(kf[k], qf[k], k == 0 ? negm : sc); PIN(sc);
      SBAR();
    }
  };
  auto phaseB = [&](const u32x4& po0, const u32x4& po1, u32x4& pn0, u32x4& pn1, auto&& rd) {
    float sacc = 0.f;
    __builtin_amdgcn_s_setprio(0);
    SBAR();
#pragma unroll
    for (int i = 0; i < 8; ++i) {
      rd(i);
      SBAR();
      const bf16x8 vf = cat(va[i], vb[i]);
      const bf16x8 pf = i < 4 ? BF(po0) : BF(po1);
      if ((i & 3) == 0) { o0 = MFMA(vf, pf, o0); PIN(o0); }
      if ((i & 3) == 1) { o1 = MFMA(vf, pf, o1); PIN(o1); }
      if ((i & 3) == 2) { o2 = MFMA(vf, pf, o2); PIN(o2); }
      if ((i & 3) == 3) { o3 = MFMA(vf, pf, o3); PIN(o3); }
      SBAR();
      const float e0 = __builtin_amdgcn_exp2f(sc[2 * i]), e1 = __builtin_amdgcn_exp2f(sc[2 * i + 1]);
      if (i == 0) sacc = e0 + e1; else { sacc += e0; sacc += e1; }
      if (i < 4) { pn0[i] = cvtpk(e0, e1); PIN(pn0); } else { pn1[i - 4] = cvtpk(e0, e1); PIN(pn1); }
      PIN(sacc);
      SBAR();
    }
    asm volatile("" ::: "memory");
    return sacc;
  };

  {
    sc = (f32x16){};
#pragma unroll
    for (int k = 0; k < 4; ++k) sc = MFMA(kf[4 * g + k], qf[4 * g + k], sc);
#pragma unroll
    for (int k = 0; k < 4; ++k) sc = MFMA(kf[4 * (1 - g) + k], qf[4 * (1 - g) + k], sc);
    PIN(sc);
    SBAR();
    o0 = (f32x16){}; o1 = (f32x16){}; o2 = (f32x16){}; o3 = (f32x16){};
    PIN(o0); PIN(o1); PIN(o2); PIN(o3);
    SBAR();
    m = rowmax(sc);
#pragma unroll
    for (int i = 0; i < 16; ++i) { negm[i] = -m; sc[i] -= m; }
    PIN(negm); PIN(sc);
    if constexpr (G) lbar(); else wait_bar<hyoung(0, 17, 16)>();
    hslot(IC(16));
#pragma unroll
    for (int k = 0; k < 8; ++k) kf[k] = lds_b128(aK[k] + HB);
    l = probs(pa0, pa1);
    PIN(pa0); PIN(pa1); PIN(l);
    wait_bar<hyoung(G, G ? 16 : 18, 17)>();
    IT(18);
  }

  const rsrc_t rsW = mkrsrc(wo + (size_t)(ks * 1024 + g * 512) * DM + nt * 128, 512 * DM * 2 - nt * 256);
  s16x4 wfa[8][4], wfb[8][4];
  auto issueW = [&](unsigned p, unsigned blk_) {
    const unsigned vw = (16 * r + l4) * 2048 + 16 * ((lane & 15) ^ (l4 << 2));
    const unsigned so = ((p + b) & 7) * (64 * 2048);
    dma4<OPOL>(dstw + blk_ * HB, vw, vw, vw, vw, rsW, so, so + 7168, so + 2 * 7168, so + 3 * 7168);
  };
  auto readW = [&](int p, int k, unsigned blk_) {
    const unsigned a_ = (baseT ^ (r << 6)) + blk_ * HB + 4096 * k;
    wfa[p][k] = lds_tr(a_); wfb[p][k] = lds_tr(a_ + 2048);
  };

  unsigned nso = (g ? 2 : 4) * HB;
  unsigned due = 0;
  if (PACE && !G) due = realtime() + PACE0;
  auto step = [&](auto jc, auto lastc) {
    constexpr int j = decltype(jc)::value;
    constexpr bool last = decltype(lastc)::value;
    constexpr bool kv = !last || G || j <= 28;
    constexpr int wpos = !last ? -1 : G ? (j == 26 ? 0 : j == 28 ? 1 : -1) : (j == 25 ? 0 : j == 27 ? 1 : j == 30 ? 2 : -1);
    constexpr int wblk = wpos == 2 ? 0 : 6 + 2 * wpos + G;
    auto issue = [&]() {
      if constexpr (kv) { issueKV(nso, (G ? j + 1 : j) % 3); nso += HB; }
      if constexpr (wpos >= 0) issueW(wpos, wblk);
    };
    u32x4& po0 = (j & 1) ? pa0 : pb0; u32x4& po1 = (j & 1) ? pa1 : pb1;
    u32x4& pn0 = (j & 1) ? pb0 : pa0; u32x4& pn1 = (j & 1) ? pb1 : pa1;
    if constexpr (G) issue();
    phaseA(((j + 2) % 3) * HB);
    const float sacc = phaseB(po0, po1, pn0, pn1, [&](int i) {
      if (i < 4) { kf[2 * i] = lds_b128(aK[2 * i] + ((j + 1) % 3) * HB); kf[2 * i + 1] = lds_b128(aK[2 * i + 1] + ((j + 1) % 3) * HB); }
    });
    if constexpr (!G) issue();
    guard(sacc, pn0, pn1);
    if constexpr (PACE && !G && j <= PEND) { due += PACE; pace(due); }
    constexpr int young = !last ? 4 : G ? (j == 25 || j == 30 ? 4 : 8) : (j <= 28 ? 8 : j == 29 ? 0 : 4);
    wait_bar<young>();
  };
  for (unsigned jj = 0; jj < 4; ++jj)
    sfor<6>([&](auto c) { step(IC(decltype(c)::value + 1), std::false_type{}); ITL(19 + 6 * jj + decltype(c)::value); });
  sfor<6>([&](auto c) { step(IC(decltype(c)::value + 25), std::true_type{}); ITL(43 + decltype(c)::value); });
  IT(48);

  {
    if constexpr (G) issueW(2, 1);
    phaseA(0);
    const float sacc = phaseB(pa0, pa1, pb0, pb1, [&](int i) { readW(i >> 2, i & 3, 6 + 2 * (i >> 2) + g); });
    guard(sacc, pb0, pb1);
    l = swap_sum(l);
    const unsigned xl = lds0 + lane * 16;
    *(LAS u32x4*)(uintptr_t)(xl + 2 * HB + wid * 2048) = pb0;
    *(LAS u32x4*)(uintptr_t)(xl + 2 * HB + wid * 2048 + 1024) = pb1;
    *(LAS f32x2*)(uintptr_t)(lds0 + 5 * HB + wid * 512 + lane * 8) = (f32x2){m, l};
    lbar();
    IT(49);
    {
      const unsigned xw = xl + 6 * HB + wid * 8192;
      const f32x16 sa = g ? o0 : o2, sb = g ? o1 : o3;
      sts_f4(xw, quad<0>(sa)); sts_f4(xw + 1024, quad<1>(sa)); sts_f4(xw + 2048, quad<2>(sa)); sts_f4(xw + 3072, quad<3>(sa));
      sts_f4(xw + 4096, quad<0>(sb)); sts_f4(xw + 5120, quad<1>(sb)); sts_f4(xw + 6144, quad<2>(sb)); sts_f4(xw + 7168, quad<3>(sb));
    }
    const unsigned pw = wid ^ 4;
    const bf16x8 pp0 = lds_b128(xl + 2 * HB + pw * 2048), pp1 = lds_b128(xl + 2 * HB + pw * 2048 + 1024);
    const f32x2 ml = *(const LAS f32x2*)(uintptr_t)(lds0 + 5 * HB + pw * 512 + lane * 8);
    const float mf = fmaxf(m, ml[0]);
    const float a0 = __builtin_amdgcn_exp2f(m - mf), a1 = __builtin_amdgcn_exp2f(ml[0] - mf);
    const float inv = 1.0f / (a0 * l + a1 * ml[1]);
    float c0 = a0 * inv, c1 = a1 * inv;
    PIN(c0); PIN(c1);
    wait_bar<0>();
    IT(50);
#pragma unroll
    for (int k = 0; k < 8; ++k) {
      const unsigned a_ = aV[2 * g + (k & 1)] + HB + 4096 * ((k >> 1) & 1) + (k >> 2) * (g ? -8192 : 8192);
      va[k] = lds_tr(a_); vb[k] = lds_tr(a_ + 2048);
    }
    f32x16 ea = g ? o2 : o0, eb = g ? o3 : o1, ua, ub;
    const unsigned xr = xl + 6 * HB + pw * 8192;
#pragma unroll
    for (int i = 0; i < 4; ++i) {
      const f32x4 t0 = lds_f4(xr + i * 1024), t1 = lds_f4(xr + 4096 + i * 1024);
#pragma unroll
      for (int e = 0; e < 4; ++e) { ua[4 * i + e] = t0[e]; ub[4 * i + e] = t1[e]; }
    }
#pragma unroll
    for (int k = 0; k < 4; ++k) readW(2, k, g);
    SBAR();
    ea = MFMA(cat(va[0], vb[0]), BF(pb0), ea); eb = MFMA(cat(va[1], vb[1]), BF(pb0), eb);
    ea = MFMA(cat(va[2], vb[2]), BF(pb1), ea); eb = MFMA(cat(va[3], vb[3]), BF(pb1), eb);
    ua = MFMA(cat(va[4], vb[4]), pp0, ua); ub = MFMA(cat(va[5], vb[5]), pp0, ub);
    ua = MFMA(cat(va[6], vb[6]), pp1, ua); ub = MFMA(cat(va[7], vb[7]), pp1, ub);
    PIN(ea); PIN(eb); PIN(ua); PIN(ub);
    SBAR();
    ea = ea * c0 + ua * c1;
    const unsigned vo = ((h * 4 + r) * 8 + 4 * g) * 1024 + lane * 16;
    __builtin_amdgcn_raw_buffer_store_b128(pack8<0>(ea), rsB, vo, 0, 16);
    __builtin_amdgcn_raw_buffer_store_b128(pack8<1>(ea), rsB, vo + 1024, 0, 16);
    eb = eb * c0 + ub * c1;
    __builtin_amdgcn_raw_buffer_store_b128(pack8<0>(eb), rsB, vo + 2048, 0, 16);
    __builtin_amdgcn_raw_buffer_store_b128(pack8<1>(eb), rsB, vo + 3072, 0, 16);
    IT(51);
    lbar();
    issueW(3, g); issueW(4, 2 + g);
    wait_bar<0>();
    IT(52);
  }
  gu64* const flag = (gu64*)(wsb + WS_FLAG);
  if constexpr (!G) if (r == 0 && lane == 0) __hip_atomic_store(flag + 32 * (1 + h), tag, __ATOMIC_RELAXED, __HIP_MEMORY_SCOPE_AGENT);
  issueW(5, 4 + g); issueW(6, 6 + g); issueW(7, 8 + g);
#pragma unroll
  for (int k = 0; k < 8; ++k) readW(3 + (k >> 2), k & 3, 2 * (k >> 2) + g);
  asm volatile("" ::: "memory");
  if constexpr (!G) if (r == 0) poll(flag + 32 * (1 + 8 * ks + (lane & 7)), tag);
  wait_bar<0>();
  IT(53);

  auto issueD = [&](unsigned s) {
    const unsigned c = (4 * (s & 1) + r + b) & 7, q = (oq + 1 + (s >> 1)) & 3;
    const unsigned so = (8 * ks + 4 * g + (c >> 1)) * 32768 + q * 8192 + (c & 1) * 4096;
    const unsigned vl = lane * 16;
    dma4<2>(dstw + (2 * (s & 3) + g) * HB, vl, vl, vl, vl, rsB, so, so, so, so);
  };
  issueD(0); issueD(1);
#pragma unroll
  for (int k = 0; k < 12; ++k) readW(5 + (k >> 2), k & 3, 4 + 2 * (k >> 2) + g);
  wait_bar<4>();
  IT(54);
  if constexpr (!G) { issueD(2); issueD(3); }
  {
    unsigned aD[2] = {lds0 + g * HB + lane * 16, lds0 + (4 + g) * HB + lane * 16};
    PIN(aD[0]); PIN(aD[1]);
    unsigned aR = lds0 + 8 * HB + r * 4096 + lane * 16;
    PIN(aR);
    bf16x8 of[8];
    f32x16 acc[2];
    f32x4 k0, k1, t0, t1;
    auto rdO = [&](int f) { of[f & 7] = lds_b128(aD[(f >> 5) & 1] + ((f >> 4) & 1) * 2 * HB + (f & 15) * 1024); };
    auto give = [&](int z) {
      const f32x16& a = acc[z & 1];
      sts_f4(aR + (z & 1) * HB + g * 2048, quad<2 - 2 * G>(a)); sts_f4(aR + (z & 1) * HB + g * 2048 + 1024, quad<3 - 2 * G>(a));
      k0 = quad<2 * G>(a); k1 = quad<2 * G + 1>(a);
    };
    auto take = [&](int z) {
      t0 = lds_f4(aR + (z & 1) * HB + (1 - g) * 2048); t1 = lds_f4(aR + (z & 1) * HB + (1 - g) * 2048 + 1024);
    };
    auto send = [&](int z) {
      const unsigned vo = WS_PART + ((nt * 4 + ((ks + 1 + z) & 3)) * 3 + z) * HB + wid * 2048 + lane * 16;
      k0 += t0; k1 += t1;
      __builtin_amdgcn_raw_buffer_store_b128(__builtin_bit_cast(u32x4, k0), rsB, vo, 0, 16);
      __builtin_amdgcn_raw_buffer_store_b128(__builtin_bit_cast(u32x4, k1), rsB, vo + 1024, 0, 16);
      asm volatile("s_nop 1" ::: "memory");
    };
#pragma unroll
    for (int f = 0; f < 6; ++f) rdO(f);
    sfor<8>([&](auto sc_) {
      constexpr int s = decltype(sc_)::value, z = s >> 1;
      sfor<16>([&](auto ic) {
        constexpr int i = decltype(ic)::value, f = 16 * s + i;
        if constexpr (i == DST && s == 0 && G) { issueD(2); issueD(3); }
        if constexpr (i == DST && s >= 1 && s < 5 && G) issueD(s + 3);
        if constexpr (i <= 9) rdO(f + 6);
        if constexpr (i == 2 && (s == 2 || s == 4 || s == 6)) give(z - 1);
        if constexpr (i == 4 && s == 6) { wait_bar<0>(); take(2); }
        if constexpr (i == 6 && s == 6) send(2);
        if constexpr (i == 8 && s == 7) {
          wait_bar<0>();
          if constexpr (!G) if (r == 0 && lane == 0) __hip_atomic_store(flag + 32 * (33 + h), tag, __ATOMIC_RELAXED, __HIP_MEMORY_SCOPE_AGENT);
        }
        if constexpr (i == 13 && s < 7) {
          if constexpr (s < 6) wait_bar<(s < 5 ? 8 : 4)>();
          if constexpr (s < 4 && !G) issueD(s + 4);
          rdO(f + 3); rdO(f + 4); rdO(f + 5);
          if constexpr (s == 2 || s == 4) take(z - 1);
        }
        if constexpr (i == 14 && s < 7) { rdO(f + 5); rdO(f + 6); }
        if constexpr (i == 15 && s < 7) { rdO(f + 6); if constexpr (s == 2 || s == 4) send(z - 1); }
        SBAR();
        constexpr int p = 4 * (s & 1) + (i >> 2), k = i & 3;
        if constexpr (i == 0 && !(s & 1)) acc[z & 1] = MFMA(cat(wfa[p][k], wfb[p][k]), of[f & 7], (f32x16){});
        else acc[z & 1] = MFMA(cat(wfa[p][k], wfb[p][k]), of[f & 7], acc[z & 1]);
        PIN(acc[z & 1]);
        SBAR();
      });
      if constexpr (s & 1) IT(55 + z);
    });
    give(3);
    if constexpr (!G) if (r == 0) poll(flag + 32 * (33 + 8 * ((ks + 1 + lane % 3) & 3) + nt), tag);
    lbar();
    IT(59);
    if constexpr (!G) if (r == 0 && h == 0 && lane == 0) __hip_atomic_store(flag, tag, __ATOMIC_RELAXED, __HIP_MEMORY_SCOPE_AGENT);
    u32x4 t[6];
    const unsigned so = WS_PART + (nt * 4 + ks) * 3 * HB + wid * 2048;
#pragma unroll
    for (int z = 0; z < 3; ++z) {
      t[2 * z] = __builtin_amdgcn_raw_buffer_load_b128(rsB, lane * 16, so + z * HB, 16);
      t[2 * z + 1] = __builtin_amdgcn_raw_buffer_load_b128(rsB, lane * 16 + 1024, so + z * HB, 16);
    }
    take(3);
    k0 += t0; k1 += t1;
#pragma unroll
    for (int z = 0; z < 3; ++z) { k0 += __builtin_bit_cast(f32x4, t[2 * z]); k1 += __builtin_bit_cast(f32x4, t[2 * z + 1]); }
    const unsigned x0 = cvtpk(k0[0], k0[1]), x1 = cvtpk(k0[2], k0[3]), y0 = cvtpk(k1[0], k1[1]), y1 = cvtpk(k1[2], k1[3]);
    auto r0 = __builtin_amdgcn_permlane32_swap(x0, y0, false, false);
    auto r1 = __builtin_amdgcn_permlane32_swap(x1, y1, false, false);
    const u32x4 w4 = {r0[0], r1[0], r0[1], r1[1]};
    __builtin_amdgcn_raw_buffer_store_b128(w4, rsO, l32 * 2048 + hi * 16 + (nt * 128 + r * 32 + g * 16) * 2, 0, 0);
  }
}

__global__ __launch_bounds__(512, 2) void mha_kernel(const uint16_t* __restrict__ x, const uint16_t* __restrict__ kc,
                                                     const uint16_t* __restrict__ vc, const uint16_t* __restrict__ wq,
                                                     const uint16_t* __restrict__ wo, uint16_t* __restrict__ out, char* __restrict__ ws) {
  extern __shared__ __attribute__((aligned(16))) char smem[];
  const unsigned wid = rfl(threadIdx.x >> 6);
  const unsigned lds0 = rfl((unsigned)(uintptr_t)smem);
  if (wid >> 2) body<1>(x, kc, vc, wq, wo, out, ws, lds0, wid & 3);
  else body<0>(x, kc, vc, wq, wo, out, ws, lds0, wid & 3);
}

extern "C" void kernel_launch(void* const* d_in, const int* in_sizes, int n_in, void* d_out, int out_size, void* d_ws,
                              size_t ws_size, hipStream_t stream) {
  (void)hipFuncSetAttribute((const void*)mha_kernel, hipFuncAttributeMaxDynamicSharedMemorySize, LDSB);
  hipLaunchKernelGGL(mha_kernel, dim3(256), dim3(512), LDSB, stream, (const uint16_t*)d_in[0], (const uint16_t*)d_in[1],
                     (const uint16_t*)d_in[2], (const uint16_t*)d_in[3], (const uint16_t*)d_in[4], (uint16_t*)d_out, (char*)d_ws);
}
```

```cpp
#include <hip/hip_runtime.h>
#include <hip/hip_bf16.h>
#include <stdint.h>
#include <utility>
#include <type_traits>

typedef float f32x16 __attribute__((ext_vector_type(16)));
typedef float f32x4 __attribute__((ext_vector_type(4)));
typedef float f32x2 __attribute__((ext_vector_type(2)));
typedef __bf16 bf16x8 __attribute__((ext_vector_type(8)));
typedef __bf16 bf16x2 __attribute__((ext_vector_type(2)));
typedef short s16x4 __attribute__((ext_vector_type(4)));
typedef unsigned u32x4 __attribute__((ext_vector_type(4)));
typedef unsigned u32x2 __attribute__((ext_vector_type(2)));
typedef unsigned long long u64;
typedef __amdgpu_buffer_rsrc_t rsrc_t;
typedef __attribute__((address_space(1))) u64 gu64;

#define LAS __attribute__((address_space(3)))
#define DI __device__ __forceinline__
#define MFMA(a, b, c) __builtin_amdgcn_mfma_f32_32x32x16_bf16((a), (b), (c), 0, 0, 0)
#define SBAR() __builtin_amdgcn_sched_barrier(0)
#define PIN(x) asm volatile("" : "+v"(x))
#define BF(x) __builtin_bit_cast(bf16x8, x)
#define IC(n) std::integral_constant<int, (n)>{}

constexpr int SQ = 128, DM = 1024, NH = 32, SK = 2048, DH = 128;
constexpr int NT = SK / 64;
constexpr unsigned HB = 16384;
constexpr int LDSB = 10 * HB;
constexpr float CEXP = 0.12751743082459868f;
constexpr float SUMLIM = 1024.0f;
constexpr unsigned WS_BATCH = 4u << 20, WS_FLAG = 1u << 20, WS_PART = WS_FLAG + 65536, WS_IDLE = 3u << 20;
constexpr unsigned SPIN_CAP = 1u << 18;

#define XPOL 0
#define WPOL 0
#define OPOL 0
#define HK0 12
#define HK1 12
#define HK2 13
#define HV0 13
#define HV1 14
#define TMASK 32
#define TSTEP 0
#define PF0 2
#define PFD 2
#define PFPOL ""
#define PACE 112
#define PEND 27
#define DST 6
#define LEAK 100000
#define ABL 0
#define PACE0 0
#define TOUCH 1

DI unsigned rfl(unsigned x) { return __builtin_amdgcn_readfirstlane(x); }
DI rsrc_t mkrsrc(const void* p, unsigned bytes) { return __builtin_amdgcn_make_buffer_rsrc((void*)p, (short)0, (int)bytes, 0x00020000); }
template <int N> DI void wait_bar() { asm volatile("s_waitcnt vmcnt(%0) lgkmcnt(0)\n\ts_barrier" ::"i"(N) : "memory"); }
DI void lbar() { asm volatile("s_waitcnt lgkmcnt(0)\n\ts_barrier" ::: "memory"); }
DI bf16x8 lds_b128(unsigned addr) { return *(const LAS bf16x8*)(uintptr_t)addr; }
DI f32x4 lds_f4(unsigned addr) { return *(const LAS f32x4*)(uintptr_t)addr; }
DI void sts_f4(unsigned addr, f32x4 v) { *(LAS f32x4*)(uintptr_t)addr = v; }
DI s16x4 lds_tr(unsigned addr) { return __builtin_amdgcn_ds_read_tr16_b64_v4i16((LAS s16x4*)(uintptr_t)addr); }
DI bf16x8 cat(s16x4 a, s16x4 b) { return __builtin_bit_cast(bf16x8, __builtin_shufflevector(a, b, 0, 1, 2, 3, 4, 5, 6, 7)); }
DI unsigned cvtpk(float lo, float hi) {
  f32x2 v = {lo, hi};
  return __builtin_bit_cast(unsigned, __builtin_convertvector(v, bf16x2));
}
template <int S> DI u32x4 pack8(const f32x16& v) {
  return (u32x4){cvtpk(v[8 * S + 0], v[8 * S + 1]), cvtpk(v[8 * S + 2], v[8 * S + 3]), cvtpk(v[8 * S + 4], v[8 * S + 5]), cvtpk(v[8 * S + 6], v[8 * S + 7])};
}
template <int S> DI f32x4 quad(const f32x16& v) { return (f32x4){v[4 * S], v[4 * S + 1], v[4 * S + 2], v[4 * S + 3]}; }
DI float swap_max(float v) {
  auto rr = __builtin_amdgcn_permlane32_swap(__float_as_uint(v), __float_as_uint(v), false, false);
  return fmaxf(__uint_as_float(rr[0]), __uint_as_float(rr[1]));
}
DI float swap_sum(float v) {
  auto rr = __builtin_amdgcn_permlane32_swap(__float_as_uint(v), __float_as_uint(v), false, false);
  return __uint_as_float(rr[0]) + __uint_as_float(rr[1]);
}
#define DMA4(pol) asm volatile("s_mov_b32 m0, %0\n\ts_nop 0\n\t" \
    "buffer_load_dwordx4 %1, %5, %6 offen" pol " lds\n\t" \
    "buffer_load_dwordx4 %2, %5, %7 offen offset:1024" pol " lds\n\t" \
    "buffer_load_dwordx4 %3, %5, %8 offen offset:2048" pol " lds\n\t" \
    "buffer_load_dwordx4 %4, %5, %9 offen offset:3072" pol " lds" \
    ::"s"(dst), "v"(va), "v"(vb), "v"(vc_), "v"(vd), "s"(rs), "s"(s0), "s"(s1), "s"(s2), "s"(s3) : "memory")
template <int POL>
DI void dma4(unsigned dst, unsigned va, unsigned vb, unsigned vc_, unsigned vd, rsrc_t rs, unsigned s0, unsigned s1, unsigned s2, unsigned s3) {
  if constexpr (POL == 1) DMA4(" nt");
  else if constexpr (POL == 2) DMA4(" sc1");
  else DMA4("");
}
template <class F, int... Is> DI void sfor_impl(F&& f, std::integer_sequence<int, Is...>) { (f(std::integral_constant<int, Is>{}), ...); }
template <int N, class F> DI void sfor(F&& f) { sfor_impl(f, std::make_integer_sequence<int, N>{}); }
DI unsigned realtime() { u64 t; asm volatile("s_memrealtime %0\n\ts_waitcnt lgkmcnt(0)" : "=s"(t)::"memory"); return (unsigned)t; }
DI void pace(unsigned& due) {
  asm volatile("1:\n\ts_memrealtime vcc\n\ts_waitcnt lgkmcnt(0)\n\ts_sub_u32 vcc_lo, vcc_lo, %0\n\ts_cmp_lt_i32 vcc_lo, 0\n\ts_cbranch_scc1 1b\n\t"
               "s_sub_u32 vcc_lo, vcc_lo, %1\n\ts_max_i32 vcc_lo, vcc_lo, 0\n\ts_add_u32 %0, %0, vcc_lo"
               : "+s"(due) : "i"(LEAK) : "memory", "scc", "vcc");
}
DI void poll(const gu64* p, u64 tag) {
#pragma unroll 1
  for (unsigned n = 0; n < SPIN_CAP; ++n) {
    const u64 v = __hip_atomic_load(p, __ATOMIC_RELAXED, __HIP_MEMORY_SCOPE_AGENT);
    if (__all(v == tag)) break;
    __builtin_amdgcn_s_sleep(2);
  }
}

constexpr int hsite(int G, int k) { return G ? (k == 0 ? HV0 : k == 1 ? HV1 : 99) : (k == 0 ? HK0 : k == 1 ? HK1 : k == 2 ? HK2 : k == 3 ? 16 : 99); }
constexpr int pfsite(int it) { return PF0 >= 0 && it >= PF0 && (it - PF0) % PFD == 0 && (it - PF0) / PFD < 4 ? (it - PF0) / PFD : -1; }
constexpr int hyoung(int G, int id, int upto) {
  int seq[64] = {}, cnt[64] = {}, n = 0;
  seq[n] = 0; cnt[n++] = 4; seq[n] = 1; cnt[n++] = 4;
  seq[n] = 90; cnt[n++] = G ? (TOUCH ? 1 : 0) : (TOUCH ? 2 : 1);
  for (int it = -1; it < upto; ++it) {
    if (it + 3 <= 15) { seq[n] = it + 3; cnt[n++] = 4; }
    if (pfsite(it) >= 0) { seq[n] = 91; cnt[n++] = 1; }
    for (int k = 0; k < 4; ++k) if (hsite(G, k) == it) { seq[n] = 16 + k; cnt[n++] = 4; }
  }
  int y = 0;
  for (int i = n - 1; i >= 0; --i) { if (seq[i] == id) return y; y += cnt[i]; }
  return -1;
}
static_assert(HK0 >= 1 && HK0 <= HK1 && HK1 <= HK2 && HK2 >= 13 && HK2 <= 14 && HV0 <= HV1 && HV1 <= 15, "head schedule");
constexpr unsigned qblock(int G, int i) { return ((i + 2) % 3 == 0 ? (G ? 5 : 2) : (i + 2) % 3 == 1 ? 6 + G : 8 + G) * HB; }

struct QFrag { bf16x8 xb[4]; s16x4 wa[4][2], wb[4][2]; };

#define IT(i)
#define ITL(i)

template <int G>
DI void body(const uint16_t* __restrict__ x, const uint16_t* __restrict__ kc, const uint16_t* __restrict__ vc, const uint16_t* __restrict__ wq,
             const uint16_t* __restrict__ wo, uint16_t* __restrict__ out, char* __restrict__ ws, unsigned lds0, unsigned r) {
  constexpr unsigned g = G;
  const unsigned lane = threadIdx.x & 63, wid = 4 * g + r;
  const unsigned l32 = lane & 31, hi = lane >> 5;
  const unsigned bid = blockIdx.x, xcd = bid & 7, idx = bid >> 3;
  const unsigned h = 4 * xcd + (idx & 3), b = idx >> 2;
  IT(0);

  const unsigned l4 = lane >> 4;
  const size_t kvoff = (size_t)(b * NH + h) * SK * DH;
  const rsrc_t rsKV = mkrsrc((g ? vc : kc) + kvoff, SK * DH * 2);
  const rsrc_t rsQ = g ? mkrsrc(wq + h * DH, DM * NH * DH * 2 - h * DH * 2) : mkrsrc(x + (size_t)b * SQ * DM, SQ * DM * 2);
  const unsigned voffA = (16 * r + l4) * 256 + 16 * ((lane & 15) ^ (g ? (l4 << 2) : (l4 | ((r & 1) << 3))));
  const unsigned voffB = g ? voffA : voffA ^ 64;
  const unsigned voffQ = g ? (16 * r + l4) * 8192 + 16 * ((lane & 15) ^ (l4 << 2))
                           : (32 * r + (lane >> 3)) * 2048 + 16 * ((lane & 7) ^ l4);
  const unsigned voffQ1 = g ? voffQ : voffQ ^ 64;
  constexpr unsigned qstS = g ? 64 * 8192 : 128, qstJ = (g ? 4 * 8192 : 8 * 2048) - 1024;
  const unsigned dstw = lds0 + r * 4096;
  const unsigned kstart = (2 * b + 4 * (h & 3)) & 15;
  auto issueQ = [&](unsigned i, unsigned blk_) {
    const unsigned so = ((i + kstart) & 15) * qstS;
    dma4<g ? WPOL : XPOL>(dstw + blk_, voffQ, voffQ1, voffQ, voffQ1, rsQ, so, so + qstJ, so + 2 * qstJ, so + 3 * qstJ);
  };
  auto issueKV = [&](unsigned so, int t3) {
    dma4<1>(dstw + (3 * g + t3) * HB, voffA, voffA, voffB, voffB, rsKV, so, so, so, so);
  };

  asm volatile("s_nop 4" ::: "memory");
  issueQ(0, qblock(G, 0)); issueQ(1, qblock(G, 1));

  const unsigned ks = h >> 3, nt = h & 7, oq = (ks + nt) & 3;
  char* const wsb = ws + (size_t)b * WS_BATCH;
  const rsrc_t rsB = mkrsrc(wsb, WS_BATCH);
  const rsrc_t rsO = mkrsrc(out + (size_t)(b * SQ + 32 * oq) * DM, 32 * DM * 2);
  if constexpr (!G) {
    const unsigned ve = lane < 2 ? WS_FLAG + 4 * lane : WS_IDLE;
    asm volatile("s_mov_b32 m0, %0\n\ts_nop 0\n\tbuffer_load_dword %1, %2, 0 offen sc1 lds" ::"s"(dstw), "v"(ve), "s"(rsB) : "memory");
    if (TOUCH) asm volatile("s_mov_b32 m0, %0\n\ts_nop 0\n\tbuffer_load_dword off, %1, 0 lds" ::"s"(dstw + 256), "s"(rsO) : "memory");
  } else if (TOUCH) {
    const rsrc_t rsT = mkrsrc(wo + (size_t)ks * 1024 * DM, 1024 * DM * 2);
    asm volatile("s_mov_b32 m0, %0\n\ts_nop 0\n\tbuffer_load_dword %1, %2, 0 offen lds" ::"s"(dstw + 3 * HB), "v"((lane & TMASK) * 32768 + r * TSTEP), "s"(rsT) : "memory");
  }

  const unsigned q4 = (lane & 15) >> 2, p4 = lane & 3, blk = (lane >> 4) & 1;
  const unsigned xrow = 32 * r + l32;
  const unsigned baseX = lds0 + 128 * xrow + 16 * (hi ^ ((xrow >> 1) & 7));
  const unsigned baseW = lds0 + 256 * (8 * hi + q4) + 16 * (4 * ((2 * g) ^ q4) + 2 * blk + (p4 & 1)) + 8 * (p4 >> 1);
  const unsigned baseK = lds0 + 8192 * g + 256 * l32 + 16 * (hi ^ ((l32 & 3) | ((l32 >> 3) << 2)));
  const unsigned baseT = lds0 + 256 * (4 * hi + q4) + 16 * (4 * q4 + 2 * blk + (p4 >> 1)) + 8 * (p4 & 1);

  f32x16 qa0 = {}, qa1 = {};
  QFrag fr[2];
  auto qiter = [&](QFrag& cur, QFrag& nxt, unsigned bx, unsigned bw, bool mm) {
    unsigned sx = baseX + bx, sw0 = baseW + bw;
    PIN(sx); PIN(sw0);
    const unsigned sw1 = sw0 ^ 64;
#pragma unroll
    for (int s = 0; s < 4; ++s) {
      nxt.xb[s] = lds_b128(sx ^ (s << 5)); nxt.wa[s][0] = lds_tr(sw0 + 4096 * s); nxt.wa[s][1] = lds_tr(sw0 + 4096 * s + 1024);
      SBAR();
      if (mm) { qa0 = MFMA(cat(cur.wa[s][0], cur.wa[s][1]), cur.xb[s], qa0); PIN(qa0); }
      SBAR();
      nxt.wb[s][0] = lds_tr(sw1 + 4096 * s); nxt.wb[s][1] = lds_tr(sw1 + 4096 * s + 1024);
      SBAR();
      if (mm) { qa1 = MFMA(cat(cur.wb[s][0], cur.wb[s][1]), cur.xb[s], qa1); PIN(qa1); }
      SBAR();
    }
  };
  auto hslot = [&](auto ic) {
    constexpr int it = decltype(ic)::value;
    if constexpr (it + 3 <= 15) issueQ(it + 3, qblock(G, it + 3));
    if constexpr (pfsite(it) >= 0) {
      const rsrc_t rsT = mkrsrc(wo + (size_t)(ks * 1024 + idx * 32 + wid * 4 + pfsite(it)) * DM + (nt >> 2) * 512, 1024);
      asm volatile("s_mov_b32 m0, %0\n\ts_nop 0\n\tbuffer_load_dwordx4 %1, %2, 0 offen" PFPOL " lds" ::"s"(dstw + (3 * g + 1) * HB), "v"(lane * 16), "s"(rsT) : "memory");
    }
    sfor<4>([&](auto kc_) {
      constexpr int k = decltype(kc_)::value;
      if constexpr (hsite(G, k) == it) issueKV(k * HB, k % 3);
    });
  };
  u64 tag = 0;
  wait_bar<hyoung(G, 0, -1)>();
  IT(1);
  qiter(fr[1], fr[0], qblock(0, 0), qblock(1, 0), false);
  hslot(IC(-1));
  sfor<15>([&](auto ic) {
    constexpr int i = decltype(ic)::value;
    wait_bar<hyoung(G, i + 1, i)>();
    ITL(i + 2);
    if constexpr (G) hslot(IC(i));
    if constexpr (!G && i == 1) {
      const u32x2 e = *(const LAS u32x2*)(uintptr_t)dstw;
      tag = (((u64)rfl(e[1]) << 32) | rfl(e[0])) + 0x9E3779B97F4A7C15ull;
      asm volatile("" : "+s"(tag));
    }
    qiter(fr[i & 1], fr[(i + 1) & 1], qblock(0, i + 1), qblock(1, i + 1), true);
    if constexpr (!G) hslot(IC(i));
  });
  bf16x8 qf[8], kf[8];
  unsigned aK[8], aV[4];
  {
    QFrag& cur = fr[1];
    const unsigned xw = lds0 + 6 * HB + wid * 4096 + lane * 16;
#pragma unroll
    for (int s = 0; s < 4; ++s) qa0 = MFMA(cat(cur.wa[s][0], cur.wa[s][1]), cur.xb[s], qa0);
    PIN(qa0);
    qa1 = MFMA(cat(cur.wb[0][0], cur.wb[0][1]), cur.xb[0], qa1);
    qa1 = MFMA(cat(cur.wb[1][0], cur.wb[1][1]), cur.xb[1], qa1); PIN(qa1);
    SBAR();
#pragma unroll
    for (int k = 0; k < 8; ++k) { aK[k] = baseK ^ (k << 5); PIN(aK[k]); }
#pragma unroll
    for (int k = 0; k < 4; ++k) { aV[k] = (baseT + 3 * HB + 8192 * g) ^ (k << 6); PIN(aV[k]); }
    qa0 *= CEXP;
    qf[4 * g + 0] = BF(pack8<0>(qa0)); PIN(qf[4 * g + 0]);
    SBAR();
    qa1 = MFMA(cat(cur.wb[2][0], cur.wb[2][1]), cur.xb[2], qa1); PIN(qa1);
    SBAR();
    qf[4 * g + 1] = BF(pack8<1>(qa0)); PIN(qf[4 * g + 1]);
    SBAR();
    qa1 = MFMA(cat(cur.wb[3][0], cur.wb[3][1]), cur.xb[3], qa1); PIN(qa1);
    SBAR();
    *(LAS bf16x8*)(uintptr_t)(xw) = qf[4 * g + 0];
    *(LAS bf16x8*)(uintptr_t)(xw + 1024) = qf[4 * g + 1];
    qa1 *= CEXP;
    qf[4 * g + 2] = BF(pack8<0>(qa1)); qf[4 * g + 3] = BF(pack8<1>(qa1));
    *(LAS bf16x8*)(uintptr_t)(xw + 2048) = qf[4 * g + 2];
    *(LAS bf16x8*)(uintptr_t)(xw + 3072) = qf[4 * g + 3];
    if constexpr (G) lbar(); else wait_bar<hyoung(0, 16, 15)>();
    IT(17);
    hslot(IC(15));
    const unsigned xr = lds0 + 6 * HB + (wid ^ 4) * 4096 + lane * 16;
#pragma unroll
    for (int k = 0; k < 4; ++k) kf[4 * g + k] = lds_b128(aK[4 * g + k]);
#pragma unroll
    for (int k = 0; k < 4; ++k) { qf[4 * (1 - g) + k] = lds_b128(xr + k * 1024); kf[4 * (1 - g) + k] = lds_b128(aK[4 * (1 - g) + k]); }
  }

  f32x16 o0, o1, o2, o3, sc, negm;
  s16x4 va[8], vb[8];
  u32x4 pa0, pa1, pb0, pb1;
  float m, l;

  auto rowmax = [&](const f32x16& s) {
    float a = fmaxf(fmaxf(s[0], s[1]), s[2]), c = fmaxf(fmaxf(s[3], s[4]), s[5]);
    a = fmaxf(fmaxf(a, s[6]), s[7]); c = fmaxf(fmaxf(c, s[8]), s[9]);
    a = fmaxf(fmaxf(a, s[10]), s[11]); c = fmaxf(fmaxf(c, s[12]), s[13]);
    a = fmaxf(fmaxf(a, s[14]), s[15]);
    return swap_max(fmaxf(a, c));
  };
  auto probs = [&](u32x4& p0, u32x4& p1) {
    float sacc = 0.f;
#pragma unroll
    for (int i = 0; i < 8; ++i) {
      const float e0 = __builtin_amdgcn_exp2f(sc[2 * i]), e1 = __builtin_amdgcn_exp2f(sc[2 * i + 1]);
      sacc += e0; sacc += e1;
      if (i < 4) p0[i] = cvtpk(e0, e1); else p1[i - 4] = cvtpk(e0, e1);
    }
    return sacc;
  };
  auto guard = [&](float sacc, u32x4& p0, u32x4& p1) {
    if (__builtin_expect(__any(!(sacc <= SUMLIM)), 0)) {
      const float dl = fmaxf(rowmax(sc), 0.f);
      const float alpha = __builtin_amdgcn_exp2f(-dl);
      m += dl;
      l *= alpha;
      o0 *= alpha; o1 *= alpha; o2 *= alpha; o3 *= alpha;
      sc -= dl; negm -= dl;
      sacc = probs(p0, p1);
      PIN(m); PIN(l); PIN(sacc); PIN(p0); PIN(p1); PIN(negm); PIN(o0); PIN(o1); PIN(o2); PIN(o3);
    }
    l += sacc;
  };
  auto phaseA = [&](unsigned vo) {
    __builtin_amdgcn_s_setprio(1);
#pragma unroll
    for (int k = 0; k < 8; ++k) {
      const unsigned a_ = aV[k & 3] + vo + 4096 * (k >> 2);
      va[k] = lds_tr(a_); vb[k] = lds_tr(a_ + 2048);
      SBAR();
      if (!ABL) sc = MFMA(kf[k], qf[k], k == 0 ? negm : sc);
      PIN(sc); if (ABL) PIN(kf[k]);
      SBAR();
    }
  };
  auto phaseB = [&](const u32x4& po0, const u32x4& po1, u32x4& pn0, u32x4& pn1, auto&& rd) {
    float sacc = 0.f;
    __builtin_amdgcn_s_setprio(0);
    SBAR();
#pragma unroll
    for (int i = 0; i < 8; ++i) {
      rd(i);
      SBAR();
      const bf16x8 vf = cat(va[i], vb[i]);
      const bf16x8 pf = i < 4 ? BF(po0) : BF(po1);
      if ((i & 3) == 0) { if (!ABL) o0 = MFMA(vf, pf, o0); else asm volatile("" :: "v"(vf), "v"(pf)); PIN(o0); }
      if ((i & 3) == 1) { if (!ABL) o1 = MFMA(vf, pf, o1); else asm volatile("" :: "v"(vf), "v"(pf)); PIN(o1); }
      if ((i & 3) == 2) { if (!ABL) o2 = MFMA(vf, pf, o2); else asm volatile("" :: "v"(vf), "v"(pf)); PIN(o2); }
      if ((i & 3) == 3) { if (!ABL) o3 = MFMA(vf, pf, o3); else asm volatile("" :: "v"(vf), "v"(pf)); PIN(o3); }
      SBAR();
      const float e0 = __builtin_amdgcn_exp2f(sc[2 * i]), e1 = __builtin_amdgcn_exp2f(sc[2 * i + 1]);
      if (i == 0) sacc = e0 + e1; else { sacc += e0; sacc += e1; }
      if (i < 4) { pn0[i] = cvtpk(e0, e1); PIN(pn0); } else { pn1[i - 4] = cvtpk(e0, e1); PIN(pn1); }
      PIN(sacc);
      SBAR();
    }
    asm volatile("" ::: "memory");
    return sacc;
  };

  {
    sc = (f32x16){};
#pragma unroll
    for (int k = 0; k < 4; ++k) sc = MFMA(kf[4 * g + k], qf[4 * g + k], sc);
#pragma unroll
    for (int k = 0; k < 4; ++k) sc = MFMA(kf[4 * (1 - g) + k], qf[4 * (1 - g) + k], sc);
    PIN(sc);
    SBAR();
    o0 = (f32x16){}; o1 = (f32x16){}; o2 = (f32x16){}; o3 = (f32x16){};
    PIN(o0); PIN(o1); PIN(o2); PIN(o3);
    SBAR();
    m = rowmax(sc);
#pragma unroll
    for (int i = 0; i < 16; ++i) { negm[i] = -m; sc[i] -= m; }
    PIN(negm); PIN(sc);
    if constexpr (G) lbar(); else wait_bar<hyoung(0, 17, 16)>();
    hslot(IC(16));
#pragma unroll
    for (int k = 0; k < 8; ++k) kf[k] = lds_b128(aK[k] + HB);
    l = probs(pa0, pa1);
    PIN(pa0); PIN(pa1); PIN(l);
    wait_bar<hyoung(G, G ? 16 : 18, 17)>();
    IT(18);
  }

  const rsrc_t rsW = mkrsrc(wo + (size_t)(ks * 1024 + g * 512) * DM + nt * 128, 512 * DM * 2 - nt * 256);
  s16x4 wfa[8][4], wfb[8][4];
  auto issueW = [&](unsigned p, unsigned blk_) {
    const unsigned vw = (16 * r + l4) * 2048 + 16 * ((lane & 15) ^ (l4 << 2));
    const unsigned so = ((p + b) & 7) * (64 * 2048);
    dma4<OPOL>(dstw + blk_ * HB, vw, vw, vw, vw, rsW, so, so + 7168, so + 2 * 7168, so + 3 * 7168);
  };
  auto readW = [&](int p, int k, unsigned blk_) {
    const unsigned a_ = (baseT ^ (r << 6)) + blk_ * HB + 4096 * k;
    wfa[p][k] = lds_tr(a_); wfb[p][k] = lds_tr(a_ + 2048);
  };

  unsigned nso = (g ? 2 : 4) * HB;
  unsigned due = 0;
  if (PACE && !G) due = realtime() + PACE0;
  auto step = [&](auto jc, auto lastc) {
    constexpr int j = decltype(jc)::value;
    constexpr bool last = decltype(lastc)::value;
    constexpr bool kv = !last || G || j <= 28;
    constexpr int wpos = !last ? -1 : G ? (j == 26 ? 0 : j == 28 ? 1 : -1) : (j == 25 ? 0 : j == 27 ? 1 : j == 30 ? 2 : -1);
    constexpr int wblk = wpos == 2 ? 0 : 6 + 2 * wpos + G;
    auto issue = [&]() {
      if constexpr (kv) { issueKV(nso, (G ? j + 1 : j) % 3); nso += HB; }
      if constexpr (wpos >= 0) issueW(wpos, wblk);
    };
    u32x4& po0 = (j & 1) ? pa0 : pb0; u32x4& po1 = (j & 1) ? pa1 : pb1;
    u32x4& pn0 = (j & 1) ? pb0 : pa0; u32x4& pn1 = (j & 1) ? pb1 : pa1;
    if constexpr (G) issue();
    phaseA(((j + 2) % 3) * HB);
    const float sacc = phaseB(po0, po1, pn0, pn1, [&](int i) {
      if (i < 4) { kf[2 * i] = lds_b128(aK[2 * i] + ((j + 1) % 3) * HB); kf[2 * i + 1] = lds_b128(aK[2 * i + 1] + ((j + 1) % 3) * HB); }
    });
    if constexpr (!G) issue();
    guard(sacc, pn0, pn1);
    if constexpr (PACE && !G && j <= PEND) { due += PACE; pace(due); }
    constexpr int young = !last ? 4 : G ? (j == 25 || j == 30 ? 4 : 8) : (j <= 28 ? 8 : j == 29 ? 0 : 4);
    wait_bar<young>();
  };
  for (unsigned jj = 0; jj < 4; ++jj)
    sfor<6>([&](auto c) { step(IC(decltype(c)::value + 1), std::false_type{}); ITL(19 + 6 * jj + decltype(c)::value); });
  sfor<6>([&](auto c) { step(IC(decltype(c)::value + 25), std::true_type{}); ITL(43 + decltype(c)::value); });
  IT(48);

  {
    if constexpr (G) issueW(2, 1);
    phaseA(0);
    const float sacc = phaseB(pa0, pa1, pb0, pb1, [&](int i) { readW(i >> 2, i & 3, 6 + 2 * (i >> 2) + g); });
    guard(sacc, pb0, pb1);
    l = swap_sum(l);
    const unsigned xl = lds0 + lane * 16;
    *(LAS u32x4*)(uintptr_t)(xl + 2 * HB + wid * 2048) = pb0;
    *(LAS u32x4*)(uintptr_t)(xl + 2 * HB + wid * 2048 + 1024) = pb1;
    *(LAS f32x2*)(uintptr_t)(lds0 + 5 * HB + wid * 512 + lane * 8) = (f32x2){m, l};
    lbar();
    IT(49);
    {
      const unsigned xw = xl + 6 * HB + wid * 8192;
      const f32x16 sa = g ? o0 : o2, sb = g ? o1 : o3;
      sts_f4(xw, quad<0>(sa)); sts_f4(xw + 1024, quad<1>(sa)); sts_f4(xw + 2048, quad<2>(sa)); sts_f4(xw + 3072, quad<3>(sa));
      sts_f4(xw + 4096, quad<0>(sb)); sts_f4(xw + 5120, quad<1>(sb)); sts_f4(xw + 6144, quad<2>(sb)); sts_f4(xw + 7168, quad<3>(sb));
    }
    const unsigned pw = wid ^ 4;
    const bf16x8 pp0 = lds_b128(xl + 2 * HB + pw * 2048), pp1 = lds_b128(xl + 2 * HB + pw * 2048 + 1024);
    const f32x2 ml = *(const LAS f32x2*)(uintptr_t)(lds0 + 5 * HB + pw * 512 + lane * 8);
    const float mf = fmaxf(m, ml[0]);
    const float a0 = __builtin_amdgcn_exp2f(m - mf), a1 = __builtin_amdgcn_exp2f(ml[0] - mf);
    const float inv = 1.0f / (a0 * l + a1 * ml[1]);
    float c0 = a0 * inv, c1 = a1 * inv;
    PIN(c0); PIN(c1);
    wait_bar<0>();
    IT(50);
#pragma unroll
    for (int k = 0; k < 8; ++k) {
      const unsigned a_ = aV[2 * g + (k & 1)] + HB + 4096 * ((k >> 1) & 1) + (k >> 2) * (g ? -8192 : 8192);
      va[k] = lds_tr(a_); vb[k] = lds_tr(a_ + 2048);
    }
    f32x16 ea = g ? o2 : o0, eb = g ? o3 : o1, ua, ub;
    const unsigned xr = xl + 6 * HB + pw * 8192;
#pragma unroll
    for (int i = 0; i < 4; ++i) {
      const f32x4 t0 = lds_f4(xr + i * 1024), t1 = lds_f4(xr + 4096 + i * 1024);
#pragma unroll
      for (int e = 0; e < 4; ++e) { ua[4 * i + e] = t0[e]; ub[4 * i + e] = t1[e]; }
    }
#pragma unroll
    for (int k = 0; k < 4; ++k) readW(2, k, g);
    SBAR();
    ea = MFMA(cat(va[0], vb[0]), BF(pb0), ea); eb = MFMA(cat(va[1], vb[1]), BF(pb0), eb);
    ea = MFMA(cat(va[2], vb[2]), BF(pb1), ea); eb = MFMA(cat(va[3], vb[3]), BF(pb1), eb);
    ua = MFMA(cat(va[4], vb[4]), pp0, ua); ub = MFMA(cat(va[5], vb[5]), pp0, ub);
    ua = MFMA(cat(va[6], vb[6]), pp1, ua); ub = MFMA(cat(va[7], vb[7]), pp1, ub);
    PIN(ea); PIN(eb); PIN(ua); PIN(ub);
    SBAR();
    ea = ea * c0 + ua * c1;
    const unsigned vo = ((h * 4 + r) * 8 + 4 * g) * 1024 + lane * 16;
    __builtin_amdgcn_raw_buffer_store_b128(pack8<0>(ea), rsB, vo, 0, 16);
    __builtin_amdgcn_raw_buffer_store_b128(pack8<1>(ea), rsB, vo + 1024, 0, 16);
    eb = eb * c0 + ub * c1;
    __builtin_amdgcn_raw_buffer_store_b128(pack8<0>(eb), rsB, vo + 2048, 0, 16);
    __builtin_amdgcn_raw_buffer_store_b128(pack8<1>(eb), rsB, vo + 3072, 0, 16);
    IT(51);
    lbar();
    issueW(3, g); issueW(4, 2 + g);
    wait_bar<0>();
    IT(52);
  }
  gu64* const flag = (gu64*)(wsb + WS_FLAG);
  if constexpr (!G) if (r == 0 && lane == 0) __hip_atomic_store(flag + 32 * (1 + h), tag, __ATOMIC_RELAXED, __HIP_MEMORY_SCOPE_AGENT);
  issueW(5, 4 + g); issueW(6, 6 + g); issueW(7, 8 + g);
#pragma unroll
  for (int k = 0; k < 8; ++k) readW(3 + (k >> 2), k & 3, 2 * (k >> 2) + g);
  asm volatile("" ::: "memory");
  if constexpr (!G) if (r == 0) poll(flag + 32 * (1 + 8 * ks + (lane & 7)), tag);
  wait_bar<0>();
  IT(53);

  auto issueD = [&](unsigned s) {
    const unsigned c = (4 * (s & 1) + r + b) & 7, q = (oq + 1 + (s >> 1)) & 3;
    const unsigned so = (8 * ks + 4 * g + (c >> 1)) * 32768 + q * 8192 + (c & 1) * 4096;
    const unsigned vl = lane * 16;
    dma4<2>(dstw + (2 * (s & 3) + g) * HB, vl, vl, vl, vl, rsB, so, so, so, so);
  };
  issueD(0); issueD(1);
#pragma unroll
  for (int k = 0; k < 12; ++k) readW(5 + (k >> 2), k & 3, 4 + 2 * (k >> 2) + g);
  wait_bar<4>();
  IT(54);
  if constexpr (!G) { issueD(2); issueD(3); }
  {
    unsigned aD[2] = {lds0 + g * HB + lane * 16, lds0 + (4 + g) * HB + lane * 16};
    PIN(aD[0]); PIN(aD[1]);
    unsigned aR = lds0 + 8 * HB + r * 4096 + lane * 16;
    PIN(aR);
    bf16x8 of[8];
    f32x16 acc[2];
    f32x4 k0, k1, t0, t1;
    auto rdO = [&](int f) { of[f & 7] = lds_b128(aD[(f >> 5) & 1] + ((f >> 4) & 1) * 2 * HB + (f & 15) * 1024); };
    auto give = [&](int z) {
      const f32x16& a = acc[z & 1];
      sts_f4(aR + (z & 1) * HB + g * 2048, quad<2 - 2 * G>(a)); sts_f4(aR + (z & 1) * HB + g * 2048 + 1024, quad<3 - 2 * G>(a));
      k0 = quad<2 * G>(a); k1 = quad<2 * G + 1>(a);
    };
    auto take = [&](int z) {
      t0 = lds_f4(aR + (z & 1) * HB + (1 - g) * 2048); t1 = lds_f4(aR + (z & 1) * HB + (1 - g) * 2048 + 1024);
    };
    auto send = [&](int z) {
      const unsigned vo = WS_PART + ((nt * 4 + ((ks + 1 + z) & 3)) * 3 + z) * HB + wid * 2048 + lane * 16;
      k0 += t0; k1 += t1;
      __builtin_amdgcn_raw_buffer_store_b128(__builtin_bit_cast(u32x4, k0), rsB, vo, 0, 16);
      __builtin_amdgcn_raw_buffer_store_b128(__builtin_bit_cast(u32x4, k1), rsB, vo + 1024, 0, 16);
      asm volatile("s_nop 1" ::: "memory");
    };
#pragma unroll
    for (int f = 0; f < 6; ++f) rdO(f);
    sfor<8>([&](auto sc_) {
      constexpr int s = decltype(sc_)::value, z = s >> 1;
      sfor<16>([&](auto ic) {
        constexpr int i = decltype(ic)::value, f = 16 * s + i;
        if constexpr (i == DST && s == 0 && G) { issueD(2); issueD(3); }
        if constexpr (i == DST && s >= 1 && s < 5 && G) issueD(s + 3);
        if constexpr (i <= 9) rdO(f + 6);
        if constexpr (i == 2 && (s == 2 || s == 4 || s == 6)) give(z - 1);
        if constexpr (i == 4 && s == 6) { wait_bar<0>(); take(2); }
        if constexpr (i == 6 && s == 6) send(2);
        if constexpr (i == 8 && s == 7) {
          wait_bar<0>();
          if constexpr (!G) if (r == 0 && lane == 0) __hip_atomic_store(flag + 32 * (33 + h), tag, __ATOMIC_RELAXED, __HIP_MEMORY_SCOPE_AGENT);
        }
        if constexpr (i == 13 && s < 7) {
          if constexpr (s < 6) wait_bar<(s < 5 ? 8 : 4)>();
          if constexpr (s < 4 && !G) issueD(s + 4);
          rdO(f + 3); rdO(f + 4); rdO(f + 5);
          if constexpr (s == 2 || s == 4) take(z - 1);
        }
        if constexpr (i == 14 && s < 7) { rdO(f + 5); rdO(f + 6); }
        if constexpr (i == 15 && s < 7) { rdO(f + 6); if constexpr (s == 2 || s == 4) send(z - 1); }
        SBAR();
        constexpr int p = 4 * (s & 1) + (i >> 2), k = i & 3;
        if constexpr (i == 0 && !(s & 1)) acc[z & 1] = MFMA(cat(wfa[p][k], wfb[p][k]), of[f & 7], (f32x16){});
        else acc[z & 1] = MFMA(cat(wfa[p][k], wfb[p][k]), of[f & 7], acc[z & 1]);
        PIN(acc[z & 1]);
        SBAR();
      });
      if constexpr (s & 1) IT(55 + z);
    });
    give(3);
    if constexpr (!G) if (r == 0) poll(flag + 32 * (33 + 8 * ((ks + 1 + lane % 3) & 3) + nt), tag);
    lbar();
    IT(59);
    if constexpr (!G) if (r == 0 && h == 0 && lane == 0) __hip_atomic_store(flag, tag, __ATOMIC_RELAXED, __HIP_MEMORY_SCOPE_AGENT);
    u32x4 t[6];
    const unsigned so = WS_PART + (nt * 4 + ks) * 3 * HB + wid * 2048;
#pragma unroll
    for (int z = 0; z < 3; ++z) {
      t[2 * z] = __builtin_amdgcn_raw_buffer_load_b128(rsB, lane * 16, so + z * HB, 16);
      t[2 * z + 1] = __builtin_amdgcn_raw_buffer_load_b128(rsB, lane * 16 + 1024, so + z * HB, 16);
    }
    take(3);
    k0 += t0; k1 += t1;
#pragma unroll
    for (int z = 0; z < 3; ++z) { k0 += __builtin_bit_cast(f32x4, t[2 * z]); k1 += __builtin_bit_cast(f32x4, t[2 * z + 1]); }
    const unsigned x0 = cvtpk(k0[0], k0[1]), x1 = cvtpk(k0[2], k0[3]), y0 = cvtpk(k1[0], k1[1]), y1 = cvtpk(k1[2], k1[3]);
    auto r0 = __builtin_amdgcn_permlane32_swap(x0, y0, false, false);
    auto r1 = __builtin_amdgcn_permlane32_swap(x1, y1, false, false);
    const u32x4 w4 = {r0[0], r1[0], r0[1], r1[1]};
    __builtin_amdgcn_raw_buffer_store_b128(w4, rsO, l32 * 2048 + hi * 16 + (nt * 128 + r * 32 + g * 16) * 2, 0, 0);
  }
}

__global__ __launch_bounds__(512, 2) void mha_kernel(const uint16_t* __restrict__ x, const uint16_t* __restrict__ kc,
                                                     const uint16_t* __restrict__ vc, const uint16_t* __restrict__ wq,
                                                     const uint16_t* __restrict__ wo, uint16_t* __restrict__ out, char* __restrict__ ws) {
  extern __shared__ __attribute__((aligned(16))) char smem[];
  const unsigned wid = rfl(threadIdx.x >> 6);
  const unsigned lds0 = rfl((unsigned)(uintptr_t)smem);
  if (wid >> 2) body<1>(x, kc, vc, wq, wo, out, ws, lds0, wid & 3);
  else body<0>(x, kc, vc, wq, wo, out, ws, lds0, wid & 3);
}

extern "C" void kernel_launch(void* const* d_in, const int* in_sizes, int n_in, void* d_out, int out_size, void* d_ws,
                              size_t ws_size, hipStream_t stream) {
  (void)hipFuncSetAttribute((const void*)mha_kernel, hipFuncAttributeMaxDynamicSharedMemorySize, LDSB);
  hipLaunchKernelGGL(mha_kernel, dim3(256), dim3(512), LDSB, stream, (const uint16_t*)d_in[0], (const uint16_t*)d_in[1],
                     (const uint16_t*)d_in[2], (const uint16_t*)d_in[3], (const uint16_t*)d_in[4], (uint16_t*)d_out, (char*)d_ws);
}
```

```cpp
#include <hip/hip_runtime.h>
#include <hip/hip_bf16.h>
#include <stdint.h>
#include <utility>
#include <type_traits>

typedef float f32x16 __attribute__((ext_vector_type(16)));
typedef float f32x4 __attribute__((ext_vector_type(4)));
typedef float f32x2 __attribute__((ext_vector_type(2)));
typedef __bf16 bf16x8 __attribute__((ext_vector_type(8)));
typedef __bf16 bf16x2 __attribute__((ext_vector_type(2)));
typedef short s16x4 __attribute__((ext_vector_type(4)));
typedef unsigned u32x4 __attribute__((ext_vector_type(4)));
typedef unsigned u32x2 __attribute__((ext_vector_type(2)));
typedef unsigned long long u64;
typedef __amdgpu_buffer_rsrc_t rsrc_t;
typedef __attribute__((address_space(1))) u64 gu64;

#define LAS __attribute__((address_space(3)))
#define DI __device__ __forceinline__
#define MFMA(a, b, c) __builtin_amdgcn_mfma_f32_32x32x16_bf16((a), (b), (c), 0, 0, 0)
#define SBAR() __builtin_amdgcn_sched_barrier(0)
#define PIN(x) asm volatile("" : "+v"(x))
#define BF(x) __builtin_bit_cast(bf16x8, x)
#define IC(n) std::integral_constant<int, (n)>{}

constexpr int SQ = 128, DM = 1024, NH = 32, SK = 2048, DH = 128;
constexpr int NT = SK / 64;
constexpr unsigned HB = 16384;
constexpr int LDSB = 10 * HB;
constexpr float CEXP = 0.12751743082459868f;
constexpr float SUMLIM = 1024.0f;
constexpr unsigned WS_BATCH = 4u << 20, WS_FLAG = 1u << 20, WS_PART = WS_FLAG + 65536, WS_IDLE = 3u << 20;
constexpr unsigned SPIN_CAP = 1u << 18;

#define XPOL 0
#define WPOL 0
#define OPOL 0
#define HK0 12
#define HK1 12
#define HK2 13
#define HV0 13
#define HV1 14
#define TMASK 32
#define TSTEP 0
#define PF0 -1
#define PFD 2
#define PFPOL ""
#define PACE 112
#define PEND 30
#define DST 6
#define LEAK 200
#define ABL 0
#define PACE0 0
#define TOUCH 1
#define HABL 0
#define NTQ 8
#define PBASE 12
#define PDEL 335
#define WEARLY 1
#define PACE2 PACE
#define PLAST 95
#define PMIN 100
#define PMINL 85
#define ROT 0
#define WROT 0
#define CROT 1
#define OQM 1
#define HP 50
#define OP 0
#define KSM 3
#define LPF 0
#define WAPOL " nt"
#define W2POL 0
#define W2A 2
#define W2B 1
#define STR 0
#define STRM 3
#define ONEP 0
#define PHS 0

DI unsigned rfl(unsigned x) { return __builtin_amdgcn_readfirstlane(x); }
DI rsrc_t mkrsrc(const void* p, unsigned bytes) { return __builtin_amdgcn_make_buffer_rsrc((void*)p, (short)0, (int)bytes, 0x00020000); }
template <int N> DI void wait_bar() { asm volatile("s_waitcnt vmcnt(%0) lgkmcnt(0)\n\ts_barrier" ::"i"(N) : "memory"); }
DI void lbar() { asm volatile("s_waitcnt lgkmcnt(0)\n\ts_barrier" ::: "memory"); }
DI bf16x8 lds_b128(unsigned addr) { return *(const LAS bf16x8*)(uintptr_t)addr; }
DI f32x4 lds_f4(unsigned addr) { return *(const LAS f32x4*)(uintptr_t)addr; }
DI void sts_f4(unsigned addr, f32x4 v) { *(LAS f32x4*)(uintptr_t)addr = v; }
DI s16x4 lds_tr(unsigned addr) { return __builtin_amdgcn_ds_read_tr16_b64_v4i16((LAS s16x4*)(uintptr_t)addr); }
DI bf16x8 cat(s16x4 a, s16x4 b) { return __builtin_bit_cast(bf16x8, __builtin_shufflevector(a, b, 0, 1, 2, 3, 4, 5, 6, 7)); }
DI unsigned cvtpk(float lo, float hi) {
  f32x2 v = {lo, hi};
  return __builtin_bit_cast(unsigned, __builtin_convertvector(v, bf16x2));
}
template <int S> DI u32x4 pack8(const f32x16& v) {
  return (u32x4){cvtpk(v[8 * S + 0], v[8 * S + 1]), cvtpk(v[8 * S + 2], v[8 * S + 3]), cvtpk(v[8 * S + 4], v[8 * S + 5]), cvtpk(v[8 * S + 6], v[8 * S + 7])};
}
template <int S> DI f32x4 quad(const f32x16& v) { return (f32x4){v[4 * S], v[4 * S + 1], v[4 * S + 2], v[4 * S + 3]}; }
DI float swap_max(float v) {
  auto rr = __builtin_amdgcn_permlane32_swap(__float_as_uint(v), __float_as_uint(v), false, false);
  return fmaxf(__uint_as_float(rr[0]), __uint_as_float(rr[1]));
}
DI float swap_sum(float v) {
  auto rr = __builtin_amdgcn_permlane32_swap(__float_as_uint(v), __float_as_uint(v), false, false);
  return __uint_as_float(rr[0]) + __uint_as_float(rr[1]);
}
#define DMA4(pol) asm volatile("s_mov_b32 m0, %0\n\ts_nop 0\n\t" \
    "buffer_load_dwordx4 %1, %5, %6 offen" pol " lds\n\t" \
    "buffer_load_dwordx4 %2, %5, %7 offen offset:1024" pol " lds\n\t" \
    "buffer_load_dwordx4 %3, %5, %8 offen offset:2048" pol " lds\n\t" \
    "buffer_load_dwordx4 %4, %5, %9 offen offset:3072" pol " lds" \
    ::"s"(dst), "v"(va), "v"(vb), "v"(vc_), "v"(vd), "s"(rs), "s"(s0), "s"(s1), "s"(s2), "s"(s3) : "memory")
template <int POL>
DI void dma4(unsigned dst, unsigned va, unsigned vb, unsigned vc_, unsigned vd, rsrc_t rs, unsigned s0, unsigned s1, unsigned s2, unsigned s3) {
  if constexpr (POL == 1) DMA4(" nt");
  else if constexpr (POL == 2) DMA4(" sc1");
  else DMA4("");
}
template <class F, int... Is> DI void sfor_impl(F&& f, std::integer_sequence<int, Is...>) { (f(std::integral_constant<int, Is>{}), ...); }
template <int N, class F> DI void sfor(F&& f) { sfor_impl(f, std::make_integer_sequence<int, N>{}); }
DI unsigned realtime() { u64 t; asm volatile("s_memrealtime %0\n\ts_waitcnt lgkmcnt(0)" : "=s"(t)::"memory"); return (unsigned)t; }
template <int PM> DI void pace(unsigned& due, unsigned& last, unsigned& pp) {
  asm volatile("s_add_u32 %0, %0, %2\n\ts_add_u32 %1, %1, %5\n\ts_sub_u32 vcc_hi, %1, %0\n\ts_cmp_gt_i32 vcc_hi, 0\n\ts_cselect_b32 %1, %1, %0\n"
               "1:\n\ts_memrealtime vcc\n\ts_waitcnt lgkmcnt(0)\n\ts_sub_u32 vcc_hi, vcc_lo, %1\n\ts_cmp_lt_i32 vcc_hi, 0\n\ts_cbranch_scc1 1b\n\t"
               "s_mov_b32 %1, vcc_lo\n\ts_sub_u32 vcc_hi, vcc_lo, %0\n\ts_cmp_gt_i32 vcc_hi, %3\n\ts_cselect_b32 %0, vcc_lo, %0\n\ts_cselect_b32 %2, %4, %2"
               : "+s"(due), "+s"(last), "+s"(pp) : "i"(LEAK), "i"(PACE2), "i"(PM) : "memory", "scc", "vcc");
}
template <int D> DI void space(unsigned& last) {
  asm volatile("s_add_u32 %0, %0, %1\n"
               "1:\n\ts_memrealtime vcc\n\ts_waitcnt lgkmcnt(0)\n\ts_sub_u32 vcc_hi, vcc_lo, %0\n\ts_cmp_lt_i32 vcc_hi, 0\n\ts_cbranch_scc1 1b\n\t"
               "s_cmp_lt_i32 vcc_hi, 6\n\ts_cselect_b32 %0, %0, vcc_lo"
               : "+s"(last) : "i"(D) : "memory", "scc", "vcc");
}
DI void poll(const gu64* p, u64 tag) {
#pragma unroll 1
  for (unsigned n = 0; n < SPIN_CAP; ++n) {
    const u64 v = __hip_atomic_load(p, __ATOMIC_RELAXED, __HIP_MEMORY_SCOPE_AGENT);
    if (__all(v == tag)) break;
    __builtin_amdgcn_s_sleep(2);
  }
}

constexpr int hsite(int G, int k) { return G ? (k == 0 ? HV0 : k == 1 ? HV1 : 99) : (k == 0 ? HK0 : k == 1 ? HK1 : k == 2 ? HK2 : k == 3 ? 16 : 99); }
constexpr int pfsite(int it) { return PF0 >= 0 && it >= PF0 && (it - PF0) % PFD == 0 && (it - PF0) / PFD < 4 ? (it - PF0) / PFD : -1; }
constexpr int hyoung(int G, int id, int upto) {
  int seq[64] = {}, cnt[64] = {}, n = 0;
  seq[n] = 0; cnt[n++] = 4; seq[n] = 1; cnt[n++] = 4;
  seq[n] = 90; cnt[n++] = G ? (TOUCH ? 1 : 0) : (TOUCH ? 2 : 1);
  for (int it = -1; it < upto; ++it) {
    if (it + 3 <= 15) { seq[n] = it + 3; cnt[n++] = 4; }
    if (pfsite(it) >= 0) { seq[n] = 91; cnt[n++] = 1; }
    for (int k = 0; k < 4; ++k) if (hsite(G, k) == it) { seq[n] = 16 + k; cnt[n++] = 4; }
  }
  int y = 0;
  for (int i = n - 1; i >= 0; --i) { if (seq[i] == id) return y; y += cnt[i]; }
  return -1;
}
static_assert(HK0 >= 1 && HK0 <= HK1 && HK1 <= HK2 && HK2 >= 13 && HK2 <= 14 && HV0 <= HV1 && HV1 <= 15, "head schedule");
constexpr unsigned qblock(int G, int i) { return ((i + 2) % 3 == 0 ? (G ? 5 : 2) : (i + 2) % 3 == 1 ? 6 + G : 8 + G) * HB; }

struct QFrag { bf16x8 xb[4]; s16x4 wa[4][2], wb[4][2]; };

#define IT(i)
#define ITL(i)

template <int G>
DI void body(const uint16_t* __restrict__ x, const uint16_t* __restrict__ kc, const uint16_t* __restrict__ vc, const uint16_t* __restrict__ wq,
             const uint16_t* __restrict__ wo, uint16_t* __restrict__ out, char* __restrict__ ws, unsigned lds0, unsigned r) {
  constexpr unsigned g = G;
  const unsigned lane = threadIdx.x & 63, wid = 4 * g + r;
  const unsigned l32 = lane & 31, hi = lane >> 5;
  const unsigned bid = blockIdx.x, xcd = bid & 7, idx = bid >> 3;
  const unsigned h = 4 * xcd + (idx & 3), b = idx >> 2;
  IT(0);

  const unsigned l4 = lane >> 4;
  const size_t kvoff = (size_t)(b * NH + h) * SK * DH;
  const rsrc_t rsKV = mkrsrc((g ? vc : kc) + kvoff, SK * DH * 2);
  const rsrc_t rsQ = g ? mkrsrc(wq + h * DH, DM * NH * DH * 2 - h * DH * 2) : mkrsrc(x + (size_t)b * SQ * DM, SQ * DM * 2);
  const unsigned voffA = (16 * r + l4) * 256 + 16 * ((lane & 15) ^ (g ? (l4 << 2) : (l4 | ((r & 1) << 3))));
  const unsigned voffB = g ? voffA : voffA ^ 64;
  const unsigned voffQ = g ? (16 * r + l4) * 8192 + 16 * ((lane & 15) ^ (l4 << 2))
                           : (32 * r + (lane >> 3)) * 2048 + 16 * ((lane & 7) ^ l4);
  const unsigned voffQ1 = g ? voffQ : voffQ ^ 64;
  constexpr unsigned qstS = g ? 64 * 8192 : 128, qstJ = (g ? 4 * 8192 : 8 * 2048) - 1024;
  const unsigned dstw = lds0 + r * 4096;
  const unsigned kstart = (KSM == 0 ? 2 * b + 4 * (h & 3) : KSM == 1 ? 2 * xcd : KSM == 2 ? 0 : KSM == 3 ? 2 * xcd + 8 * ((b ^ h) & 1) : KSM == 4 ? 2 * xcd + 4 * ((b + h) & 3) : 8 * ((b ^ h) & 1)) & 15;
  auto issueQ = [&](unsigned i, unsigned blk_) {
    const unsigned so = ((i + kstart) & 15) * qstS;
    if (g && i < NTQ) dma4<1>(dstw + blk_, voffQ, voffQ1, voffQ, voffQ1, rsQ, so, so + qstJ, so + 2 * qstJ, so + 3 * qstJ);
    else dma4<g ? WPOL : XPOL>(dstw + blk_, voffQ, voffQ1, voffQ, voffQ1, rsQ, so, so + qstJ, so + 2 * qstJ, so + 3 * qstJ);
  };
  const unsigned rot = ROT == 1 ? (idx + 5 * xcd) * HB : ROT == 2 ? (idx & 1) * HB : ROT == 3 ? (idx & 7) * HB : 0;
  auto issueKV = [&](unsigned so, int t3) {
    if (ROT) so = (so + rot) & (SK * DH * 2 - 1);
    dma4<1>(dstw + (3 * g + t3) * HB, voffA, voffA, voffB, voffB, rsKV, so, so, so, so);
  };

  asm volatile("s_nop 4" ::: "memory");
  issueQ(0, qblock(G, 0)); issueQ(1, qblock(G, 1));

  const unsigned ks = h >> 3, nt = h & 7, oq = OQM == 0 ? (ks + nt) & 3 : OQM == 1 ? ks : (ks + (nt & 2)) & 3;
  char* const wsb = ws + (size_t)b * WS_BATCH;
  const rsrc_t rsB = mkrsrc(wsb, WS_BATCH);
  const rsrc_t rsO = mkrsrc(out + (size_t)(b * SQ + 32 * oq) * DM, 32 * DM * 2);
  if constexpr (!G) {
    const unsigned ve = lane < 2 ? WS_FLAG + 4 * lane : WS_IDLE;
    asm volatile("s_mov_b32 m0, %0\n\ts_nop 0\n\tbuffer_load_dword %1, %2, 0 offen sc1 lds" ::"s"(dstw), "v"(ve), "s"(rsB) : "memory");
    if (TOUCH) asm volatile("s_mov_b32 m0, %0\n\ts_nop 0\n\tbuffer_load_dword off, %1, 0 lds" ::"s"(dstw + 256), "s"(rsO) : "memory");
  } else if (TOUCH) {
    const rsrc_t rsT = mkrsrc(wo + (size_t)ks * 1024 * DM, 1024 * DM * 2);
    asm volatile("s_mov_b32 m0, %0\n\ts_nop 0\n\tbuffer_load_dword %1, %2, 0 offen lds" ::"s"(dstw + 3 * HB), "v"((lane & TMASK) * 32768 + r * TSTEP), "s"(rsT) : "memory");
  }

  const unsigned q4 = (lane & 15) >> 2, p4 = lane & 3, blk = (lane >> 4) & 1;
  const unsigned xrow = 32 * r + l32;
  const unsigned baseX = lds0 + 128 * xrow + 16 * (hi ^ ((xrow >> 1) & 7));
  const unsigned baseW = lds0 + 256 * (8 * hi + q4) + 16 * (4 * ((2 * g) ^ q4) + 2 * blk + (p4 & 1)) + 8 * (p4 >> 1);
  const unsigned baseK = lds0 + 8192 * g + 256 * l32 + 16 * (hi ^ ((l32 & 3) | ((l32 >> 3) << 2)));
  const unsigned baseT = lds0 + 256 * (4 * hi + q4) + 16 * (4 * q4 + 2 * blk + (p4 >> 1)) + 8 * (p4 & 1);

  f32x16 qa0 = {}, qa1 = {};
  QFrag fr[2];
  auto qiter = [&](QFrag& cur, QFrag& nxt, unsigned bx, unsigned bw, bool mm) {
    unsigned sx = baseX + bx, sw0 = baseW + bw;
    PIN(sx); PIN(sw0);
    const unsigned sw1 = sw0 ^ 64;
#pragma unroll
    for (int s = 0; s < 4; ++s) {
      if (!(HABL & 2) || !mm) { nxt.xb[s] = lds_b128(sx ^ (s << 5)); nxt.wa[s][0] = lds_tr(sw0 + 4096 * s); nxt.wa[s][1] = lds_tr(sw0 + 4096 * s + 1024); }
      else { nxt.xb[s] = cur.xb[s]; nxt.wa[s][0] = cur.wa[s][0]; nxt.wa[s][1] = cur.wa[s][1]; }
      SBAR();
      if (mm) { if (!(HABL & 1)) qa0 = MFMA(cat(cur.wa[s][0], cur.wa[s][1]), cur.xb[s], qa0); else asm volatile("" :: "v"(cur.wa[s][0]), "v"(cur.wa[s][1]), "v"(cur.xb[s])); PIN(qa0); }
      SBAR();
      if (!(HABL & 2) || !mm) { nxt.wb[s][0] = lds_tr(sw1 + 4096 * s); nxt.wb[s][1] = lds_tr(sw1 + 4096 * s + 1024); }
      else { nxt.wb[s][0] = cur.wb[s][0]; nxt.wb[s][1] = cur.wb[s][1]; }
      SBAR();
      if (mm) { if (!(HABL & 1)) qa1 = MFMA(cat(cur.wb[s][0], cur.wb[s][1]), cur.xb[s], qa1); else asm volatile("" :: "v"(cur.wb[s][0]), "v"(cur.wb[s][1])); PIN(qa1); }
      SBAR();
    }
  };
  auto hslot = [&](auto ic) {
    constexpr int it = decltype(ic)::value;
    if constexpr (it + 3 <= 15 && !(HABL & 4)) issueQ(it + 3, qblock(G, it + 3));
    if constexpr (pfsite(it) >= 0) {
      const rsrc_t rsT = mkrsrc(wo + (size_t)(ks * 1024 + idx * 32 + wid * 4 + pfsite(it)) * DM + (nt >> 2) * 512, 1024);
      asm volatile("s_mov_b32 m0, %0\n\ts_nop 0\n\tbuffer_load_dwordx4 %1, %2, 0 offen" PFPOL " lds" ::"s"(dstw + (3 * g + 1) * HB), "v"(lane * 16), "s"(rsT) : "memory");
    }
    sfor<4>([&](auto kc_) {
      constexpr int k = decltype(kc_)::value;
      if constexpr (hsite(G, k) == it) issueKV(k * HB, k % 3);
    });
  };
  u64 tag = 0;
  unsigned due = 0, prev = 0, conv = 0, pp = PACE;
  wait_bar<hyoung(G, 0, -1)>();
  IT(1);
  qiter(fr[1], fr[0], qblock(0, 0), qblock(1, 0), false);
  hslot(IC(-1));
  sfor<15>([&](auto ic) {
    constexpr int i = decltype(ic)::value;
    wait_bar<hyoung(G, i + 1, i)>();
    ITL(i + 2);
    if constexpr (G) hslot(IC(i));
    if constexpr (!G && PACE && i == PBASE) {
      due = realtime() + PDEL;
      if (PHS) { const unsigned ph = (((bid * 37u) & 15u) * 8u - due) & 127u; due += ph; if (PHS > 1) conv = (ph * 43) >> 8; }
      prev = due - 1000;
    }
    if constexpr (!G && i == 1) {
      const u32x2 e = *(const LAS u32x2*)(uintptr_t)dstw;
      tag = (((u64)rfl(e[1]) << 32) | rfl(e[0])) + 0x9E3779B97F4A7C15ull;
      asm volatile("" : "+s"(tag));
    }
    qiter(fr[i & 1], fr[(i + 1) & 1], qblock(0, i + 1), qblock(1, i + 1), true);
    if constexpr (!G) hslot(IC(i));
    if constexpr (!G && HP && i == 0) prev = realtime();
    if constexpr (!G && HP && i > 0 && i < PBASE) space<HP>(prev);
  });
  bf16x8 qf[8], kf[8];
  unsigned aK[8], aV[4];
  {
    QFrag& cur = fr[1];
    const unsigned xw = lds0 + 6 * HB + wid * 4096 + lane * 16;
#pragma unroll
    for (int s = 0; s < 4; ++s) qa0 = MFMA(cat(cur.wa[s][0], cur.wa[s][1]), cur.xb[s], qa0);
    PIN(qa0);
    qa1 = MFMA(cat(cur.wb[0][0], cur.wb[0][1]), cur.xb[0], qa1);
    qa1 = MFMA(cat(cur.wb[1][0], cur.wb[1][1]), cur.xb[1], qa1); PIN(qa1);
    SBAR();
#pragma unroll
    for (int k = 0; k < 8; ++k) { aK[k] = baseK ^ (k << 5); PIN(aK[k]); }
#pragma unroll
    for (int k = 0; k < 4; ++k) { aV[k] = (baseT + 3 * HB + 8192 * g) ^ (k << 6); PIN(aV[k]); }
    qa0 *= CEXP;
    qf[4 * g + 0] = BF(pack8<0>(qa0)); PIN(qf[4 * g + 0]);
    SBAR();
    qa1 = MFMA(cat(cur.wb[2][0], cur.wb[2][1]), cur.xb[2], qa1); PIN(qa1);
    SBAR();
    qf[4 * g + 1] = BF(pack8<1>(qa0)); PIN(qf[4 * g + 1]);
    SBAR();
    qa1 = MFMA(cat(cur.wb[3][0], cur.wb[3][1]), cur.xb[3], qa1); PIN(qa1);
    SBAR();
    *(LAS bf16x8*)(uintptr_t)(xw) = qf[4 * g + 0];
    *(LAS bf16x8*)(uintptr_t)(xw + 1024) = qf[4 * g + 1];
    qa1 *= CEXP;
    qf[4 * g + 2] = BF(pack8<0>(qa1)); qf[4 * g + 3] = BF(pack8<1>(qa1));
    *(LAS bf16x8*)(uintptr_t)(xw + 2048) = qf[4 * g + 2];
    *(LAS bf16x8*)(uintptr_t)(xw + 3072) = qf[4 * g + 3];
    if constexpr (G) lbar(); else wait_bar<hyoung(0, 16, 15)>();
    IT(17);
    hslot(IC(15));
    const unsigned xr = lds0 + 6 * HB + (wid ^ 4) * 4096 + lane * 16;
#pragma unroll
    for (int k = 0; k < 4; ++k) kf[4 * g + k] = lds_b128(aK[4 * g + k]);
#pragma unroll
    for (int k = 0; k < 4; ++k) { qf[4 * (1 - g) + k] = lds_b128(xr + k * 1024); kf[4 * (1 - g) + k] = lds_b128(aK[4 * (1 - g) + k]); }
  }

  f32x16 o0, o1, o2, o3, sc, negm;
  s16x4 va[8], vb[8];
  u32x4 pa0, pa1, pb0, pb1;
  float m, l;

  auto rowmax = [&](const f32x16& s) {
    float a = fmaxf(fmaxf(s[0], s[1]), s[2]), c = fmaxf(fmaxf(s[3], s[4]), s[5]);
    a = fmaxf(fmaxf(a, s[6]), s[7]); c = fmaxf(fmaxf(c, s[8]), s[9]);
    a = fmaxf(fmaxf(a, s[10]), s[11]); c = fmaxf(fmaxf(c, s[12]), s[13]);
    a = fmaxf(fmaxf(a, s[14]), s[15]);
    return swap_max(fmaxf(a, c));
  };
  auto probs = [&](u32x4& p0, u32x4& p1) {
    float sacc = 0.f;
#pragma unroll
    for (int i = 0; i < 8; ++i) {
      const float e0 = __builtin_amdgcn_exp2f(sc[2 * i]), e1 = __builtin_amdgcn_exp2f(sc[2 * i + 1]);
      sacc += e0; sacc += e1;
      if (i < 4) p0[i] = cvtpk(e0, e1); else p1[i - 4] = cvtpk(e0, e1);
    }
    return sacc;
  };
  auto guard = [&](float sacc, u32x4& p0, u32x4& p1) {
    if (__builtin_expect(__any(!(sacc <= SUMLIM)), 0)) {
      const float dl = fmaxf(rowmax(sc), 0.f);
      const float alpha = __builtin_amdgcn_exp2f(-dl);
      m += dl;
      l *= alpha;
      o0 *= alpha; o1 *= alpha; o2 *= alpha; o3 *= alpha;
      sc -= dl; negm -= dl;
      sacc = probs(p0, p1);
      PIN(m); PIN(l); PIN(sacc); PIN(p0); PIN(p1); PIN(negm); PIN(o0); PIN(o1); PIN(o2); PIN(o3);
    }
    l += sacc;
  };
  auto phaseA = [&](unsigned vo) {
    __builtin_amdgcn_s_setprio(1);
#pragma unroll
    for (int k = 0; k < 8; ++k) {
      const unsigned a_ = aV[k & 3] + vo + 4096 * (k >> 2);
      va[k] = lds_tr(a_); vb[k] = lds_tr(a_ + 2048);
      SBAR();
      if (!ABL) sc = MFMA(kf[k], qf[k], k == 0 ? negm : sc);
      PIN(sc); if (ABL) PIN(kf[k]);
      if (STR && (k & STRM) == STRM) __builtin_amdgcn_s_sleep(STR);
      SBAR();
    }
  };
  auto phaseB = [&](const u32x4& po0, const u32x4& po1, u32x4& pn0, u32x4& pn1, auto&& rd) {
    float sacc = 0.f;
    __builtin_amdgcn_s_setprio(0);
    SBAR();
#pragma unroll
    for (int i = 0; i < 8; ++i) {
      rd(i);
      SBAR();
      const bf16x8 vf = cat(va[i], vb[i]);
      const bf16x8 pf = i < 4 ? BF(po0) : BF(po1);
      if ((i & 3) == 0) { if (!ABL) o0 = MFMA(vf, pf, o0); else asm volatile("" :: "v"(vf), "v"(pf)); PIN(o0); }
      if ((i & 3) == 1) { if (!ABL) o1 = MFMA(vf, pf, o1); else asm volatile("" :: "v"(vf), "v"(pf)); PIN(o1); }
      if ((i & 3) == 2) { if (!ABL) o2 = MFMA(vf, pf, o2); else asm volatile("" :: "v"(vf), "v"(pf)); PIN(o2); }
      if ((i & 3) == 3) { if (!ABL) o3 = MFMA(vf, pf, o3); else asm volatile("" :: "v"(vf), "v"(pf)); PIN(o3); }
      SBAR();
      const float e0 = __builtin_amdgcn_exp2f(sc[2 * i]), e1 = __builtin_amdgcn_exp2f(sc[2 * i + 1]);
      if (i == 0) sacc = e0 + e1; else { sacc += e0; sacc += e1; }
      if (i < 4) { pn0[i] = cvtpk(e0, e1); PIN(pn0); } else { pn1[i - 4] = cvtpk(e0, e1); PIN(pn1); }
      PIN(sacc);
      if (STR && (i & STRM) == STRM) __builtin_amdgcn_s_sleep(STR);
      SBAR();
    }
    asm volatile("" ::: "memory");
    return sacc;
  };

  {
    sc = (f32x16){};
#pragma unroll
    for (int k = 0; k < 4; ++k) sc = MFMA(kf[4 * g + k], qf[4 * g + k], sc);
#pragma unroll
    for (int k = 0; k < 4; ++k) sc = MFMA(kf[4 * (1 - g) + k], qf[4 * (1 - g) + k], sc);
    PIN(sc);
    SBAR();
    o0 = (f32x16){}; o1 = (f32x16){}; o2 = (f32x16){}; o3 = (f32x16){};
    PIN(o0); PIN(o1); PIN(o2); PIN(o3);
    SBAR();
    m = rowmax(sc);
#pragma unroll
    for (int i = 0; i < 16; ++i) { negm[i] = -m; sc[i] -= m; }
    PIN(negm); PIN(sc);
    if constexpr (G) lbar(); else wait_bar<hyoung(0, 17, 16)>();
    hslot(IC(16));
#pragma unroll
    for (int k = 0; k < 8; ++k) kf[k] = lds_b128(aK[k] + HB);
    l = probs(pa0, pa1);
    PIN(pa0); PIN(pa1); PIN(l);
    wait_bar<hyoung(G, G ? 16 : 18, 17)>();
    IT(18);
  }

  const rsrc_t rsW = mkrsrc(wo + (size_t)(ks * 1024 + g * 512) * DM + nt * 128, (1024 - g * 512) * DM * 2 - nt * 256);
  s16x4 wfa[8][4], wfb[8][4];
  const unsigned b6 = b >= 6 ? b - 6 : b;
  auto chunk = [&](unsigned p) { const unsigned c = p + b6; return CROT == 2 ? p : CROT ? (p < 2 ? p : c >= 8 ? c - 6 : c) : (p + b) & 7; };
  auto issueW = [&](unsigned p, unsigned blk_, unsigned half = 0) {
    const unsigned vw = (16 * r + l4) * 2048 + 16 * ((lane & 15) ^ (l4 << 2));
    const unsigned so = (chunk(p) + 8 * half) * (64 * 2048);
    if (p <= 2) dma4<W2POL>(dstw + blk_ * HB, vw, vw, vw, vw, rsW, so, so + 7168, so + 2 * 7168, so + 3 * 7168);
    else dma4<OPOL>(dstw + blk_ * HB, vw, vw, vw, vw, rsW, so, so + 7168, so + 2 * 7168, so + 3 * 7168);
  };
  auto readW = [&](int p, int k, unsigned blk_) {
    const unsigned a_ = (baseT ^ (r << 6)) + blk_ * HB + 4096 * k;
    wfa[p][k] = lds_tr(a_); wfb[p][k] = lds_tr(a_ + 2048);
  };

  unsigned nso = (g ? 2 : 4) * HB;
  if (PACE && !G && PBASE < 0) { due = realtime() + PACE0; prev = due - 1000; }
  unsigned pfo = 0, wcnt = 0, wso = chunk(0) * (64 * 2048), wdst = dstw + (6 + g) * HB;
  auto step = [&](auto jc, auto lastc) {
    constexpr int j = decltype(jc)::value;
    constexpr bool last = decltype(lastc)::value;
    constexpr bool kv = !last || G || j <= 28;
    constexpr bool wp = WEARLY == 1 && !last && j % 3 == (G ? 0 : 2);
    constexpr bool pf = LPF && !last && j == (G ? 1 : 4);
    auto issue = [&]() {
      if constexpr (pf) {
        const rsrc_t rsT = mkrsrc(wo + (size_t)(ks * 1024 + g * 512 + 128 + (idx * 4 + r) * 3) * DM + (nt >> 2) * 512, 2 * 2048 + 1024);
        asm volatile("s_mov_b32 m0, %0\n\ts_nop 0\n\tbuffer_load_dwordx4 %1, %2, %3 offen" PFPOL " lds" ::"s"(dstw + (8 + g) * HB + 3072), "v"(lane * 16), "s"(rsT), "s"(pfo) : "memory");
        pfo += 2048;
      }
      if constexpr (wp) {
        const unsigned vw = (16 * r + l4) * 2048 + 16 * ((lane & 15) ^ (l4 << 2));
        const unsigned pc = WROT ? (wcnt + r) & 3 : wcnt & 3;
        asm volatile("s_mov_b32 m0, %0\n\ts_nop 0\n\tbuffer_load_dwordx4 %1, %2, %3 offen" WAPOL " lds" ::"s"(wdst + pc * 1024), "v"(vw), "s"(rsW), "s"(wso + pc * 8192) : "memory");
        ++wcnt;
      }
      if constexpr (kv) { issueKV(nso, (G ? j + 1 : j) % 3); nso += HB; }
    };
    u32x4& po0 = (j & 1) ? pa0 : pb0; u32x4& po1 = (j & 1) ? pa1 : pb1;
    u32x4& pn0 = (j & 1) ? pb0 : pa0; u32x4& pn1 = (j & 1) ? pb1 : pa1;
    if constexpr (G) issue();
    constexpr int w2 = WEARLY == 1 && !G && last && j >= 29 ? (j == 29 ? W2A : W2B) : -1;
    if constexpr (w2 == 0) issueW(2, j == 29 ? 2 : 0, j - 29);
    if constexpr (WEARLY == 2 && !G && last && j >= 29) { issueW(j - 29, 6 + 2 * (j - 29), 0); issueW(j - 29, 7 + 2 * (j - 29), 1); }
    phaseA(((j + 2) % 3) * HB);
    if constexpr (w2 == 1) issueW(2, j == 29 ? 2 : 0, j - 29);
    const float sacc = phaseB(po0, po1, pn0, pn1, [&](int i) {
      if (i < 4) { kf[2 * i] = lds_b128(aK[2 * i] + ((j + 1) % 3) * HB); kf[2 * i + 1] = lds_b128(aK[2 * i + 1] + ((j + 1) % 3) * HB); }
    });
    if constexpr (!G) issue();
    if constexpr (w2 == 2) issueW(2, j == 29 ? 2 : 0, j - 29);
    guard(sacc, pn0, pn1);
    if constexpr (PACE && !G && j <= PEND) {
      if constexpr (last && j == 25) pp = PACE - conv;
      if constexpr (last && j >= 28) pp = PLAST - conv;
      if (!ONEP || r == 0) pace<(last ? PMINL : PMIN)>(due, prev, pp);
    }
    constexpr int young = wp || pf ? 5 : WEARLY == 2 && !G && j >= 29 ? (j == 29 ? 8 : 0) : !G && j == 30 ? 8 : 4;
    wait_bar<young>();
  };
  for (unsigned jj = 0; jj < 4; ++jj) {
    sfor<6>([&](auto c) { step(IC(decltype(c)::value + 1), std::false_type{}); ITL(19 + 6 * jj + decltype(c)::value); });
    if (jj == 1) { wso = chunk(1) * (64 * 2048); wdst = dstw + (8 + g) * HB; }
  }
  sfor<6>([&](auto c) { step(IC(decltype(c)::value + 25), std::true_type{}); ITL(43 + decltype(c)::value); });
  IT(48);

  {
    if constexpr (WEARLY == 2) issueW(2, 2 - 2 * g);
    phaseA(0);
    const float sacc = phaseB(pa0, pa1, pb0, pb1, [&](int i) { readW(i >> 2, i & 3, 6 + 2 * (i >> 2) + g); });
    guard(sacc, pb0, pb1);
    l = swap_sum(l);
    const unsigned xl = lds0 + lane * 16;
    *(LAS u32x4*)(uintptr_t)(xl + HB + wid * 2048) = pb0;
    *(LAS u32x4*)(uintptr_t)(xl + HB + wid * 2048 + 1024) = pb1;
    *(LAS f32x2*)(uintptr_t)(lds0 + 5 * HB + wid * 512 + lane * 8) = (f32x2){m, l};
    lbar();
    IT(49);
    {
      const unsigned xw = xl + 6 * HB + wid * 8192;
      const f32x16 sa = g ? o0 : o2, sb = g ? o1 : o3;
      sts_f4(xw, quad<0>(sa)); sts_f4(xw + 1024, quad<1>(sa)); sts_f4(xw + 2048, quad<2>(sa)); sts_f4(xw + 3072, quad<3>(sa));
      sts_f4(xw + 4096, quad<0>(sb)); sts_f4(xw + 5120, quad<1>(sb)); sts_f4(xw + 6144, quad<2>(sb)); sts_f4(xw + 7168, quad<3>(sb));
    }
    const unsigned pw = wid ^ 4;
    const bf16x8 pp0 = lds_b128(xl + HB + pw * 2048), pp1 = lds_b128(xl + HB + pw * 2048 + 1024);
    const f32x2 ml = *(const LAS f32x2*)(uintptr_t)(lds0 + 5 * HB + pw * 512 + lane * 8);
    const float mf = fmaxf(m, ml[0]);
    const float a0 = __builtin_amdgcn_exp2f(m - mf), a1 = __builtin_amdgcn_exp2f(ml[0] - mf);
    const float inv = 1.0f / (a0 * l + a1 * ml[1]);
    float c0 = a0 * inv, c1 = a1 * inv;
    PIN(c0); PIN(c1);
    wait_bar<0>();
    IT(50);
#pragma unroll
    for (int k = 0; k < 8; ++k) {
      const unsigned a_ = aV[2 * g + (k & 1)] + HB + 4096 * ((k >> 1) & 1) + (k >> 2) * (g ? -8192 : 8192);
      va[k] = lds_tr(a_); vb[k] = lds_tr(a_ + 2048);
    }
    f32x16 ea = g ? o2 : o0, eb = g ? o3 : o1, ua, ub;
    const unsigned xr = xl + 6 * HB + pw * 8192;
#pragma unroll
    for (int i = 0; i < 4; ++i) {
      const f32x4 t0 = lds_f4(xr + i * 1024), t1 = lds_f4(xr + 4096 + i * 1024);
#pragma unroll
      for (int e = 0; e < 4; ++e) { ua[4 * i + e] = t0[e]; ub[4 * i + e] = t1[e]; }
    }
#pragma unroll
    for (int k = 0; k < 4; ++k) readW(2, k, 2 - 2 * g);
    SBAR();
    ea = MFMA(cat(va[0], vb[0]), BF(pb0), ea); eb = MFMA(cat(va[1], vb[1]), BF(pb0), eb);
    ea = MFMA(cat(va[2], vb[2]), BF(pb1), ea); eb = MFMA(cat(va[3], vb[3]), BF(pb1), eb);
    ua = MFMA(cat(va[4], vb[4]), pp0, ua); ub = MFMA(cat(va[5], vb[5]), pp0, ub);
    ua = MFMA(cat(va[6], vb[6]), pp1, ua); ub = MFMA(cat(va[7], vb[7]), pp1, ub);
    PIN(ea); PIN(eb); PIN(ua); PIN(ub);
    SBAR();
    ea = ea * c0 + ua * c1;
    const unsigned vo = ((h * 4 + r) * 8 + 4 * g) * 1024 + lane * 16;
    __builtin_amdgcn_raw_buffer_store_b128(pack8<0>(ea), rsB, vo, 0, 16);
    __builtin_amdgcn_raw_buffer_store_b128(pack8<1>(ea), rsB, vo + 1024, 0, 16);
    eb = eb * c0 + ub * c1;
    __builtin_amdgcn_raw_buffer_store_b128(pack8<0>(eb), rsB, vo + 2048, 0, 16);
    __builtin_amdgcn_raw_buffer_store_b128(pack8<1>(eb), rsB, vo + 3072, 0, 16);
    IT(51);
    lbar();
    issueW(3, g); issueW(4, 2 + g);
    wait_bar<0>();
    IT(52);
  }
  gu64* const flag = (gu64*)(wsb + WS_FLAG);
  if constexpr (!G) if (r == 0 && lane == 0) __hip_atomic_store(flag + 32 * (1 + h), tag, __ATOMIC_RELAXED, __HIP_MEMORY_SCOPE_AGENT);
  issueW(5, 4 + g); issueW(6, 6 + g); issueW(7, 8 + g);
#pragma unroll
  for (int k = 0; k < 8; ++k) readW(3 + (k >> 2), k & 3, 2 * (k >> 2) + g);
  asm volatile("" ::: "memory");
  if constexpr (!G) if (r == 0) poll(flag + 32 * (1 + 8 * ks + (lane & 7)), tag);
  wait_bar<0>();
  IT(53);

  auto issueD = [&](unsigned s) {
    const unsigned c = chunk(4 * (s & 1) + r), q = (oq + 1 + (s >> 1)) & 3;
    const unsigned so = (8 * ks + 4 * g + (c >> 1)) * 32768 + q * 8192 + (c & 1) * 4096;
    const unsigned vl = lane * 16;
    dma4<2>(dstw + (2 * (s & 3) + g) * HB, vl, vl, vl, vl, rsB, so, so, so, so);
  };
  issueD(0); issueD(1);
#pragma unroll
  for (int k = 0; k < 12; ++k) readW(5 + (k >> 2), k & 3, 4 + 2 * (k >> 2) + g);
  wait_bar<4>();
  IT(54);
  if constexpr (!G) { issueD(2); issueD(3); }
  {
    unsigned aD[2] = {lds0 + g * HB + lane * 16, lds0 + (4 + g) * HB + lane * 16};
    PIN(aD[0]); PIN(aD[1]);
    unsigned aR = lds0 + 8 * HB + r * 4096 + lane * 16;
    PIN(aR);
    bf16x8 of[8];
    f32x16 acc[2];
    f32x4 k0, k1, t0, t1;
    auto rdO = [&](int f) { of[f & 7] = lds_b128(aD[(f >> 5) & 1] + ((f >> 4) & 1) * 2 * HB + (f & 15) * 1024); };
    auto give = [&](int z) {
      const f32x16& a = acc[z & 1];
      sts_f4(aR + (z & 1) * HB + g * 2048, quad<2 - 2 * G>(a)); sts_f4(aR + (z & 1) * HB + g * 2048 + 1024, quad<3 - 2 * G>(a));
      k0 = quad<2 * G>(a); k1 = quad<2 * G + 1>(a);
    };
    auto take = [&](int z) {
      t0 = lds_f4(aR + (z & 1) * HB + (1 - g) * 2048); t1 = lds_f4(aR + (z & 1) * HB + (1 - g) * 2048 + 1024);
    };
    auto send = [&](int z) {
      const unsigned vo = WS_PART + ((nt * 4 + ((ks + 1 + z) & 3)) * 3 + z) * HB + wid * 2048 + lane * 16;
      k0 += t0; k1 += t1;
      __builtin_amdgcn_raw_buffer_store_b128(__builtin_bit_cast(u32x4, k0), rsB, vo, 0, 16);
      __builtin_amdgcn_raw_buffer_store_b128(__builtin_bit_cast(u32x4, k1), rsB, vo + 1024, 0, 16);
      asm volatile("s_nop 1" ::: "memory");
    };
#pragma unroll
    for (int f = 0; f < 6; ++f) rdO(f);
    sfor<8>([&](auto sc_) {
      constexpr int s = decltype(sc_)::value, z = s >> 1;
      sfor<16>([&](auto ic) {
        constexpr int i = decltype(ic)::value, f = 16 * s + i;
        if constexpr (i == DST && s == 0 && G) { issueD(2); issueD(3); }
        if constexpr (i == DST && s >= 1 && s < 5 && G) issueD(s + 3);
        if constexpr (i <= 9) rdO(f + 6);
        if constexpr (i == 2 && (s == 2 || s == 4 || s == 6)) give(z - 1);
        if constexpr (i == 4 && s == 6) { wait_bar<0>(); take(2); }
        if constexpr (i == 6 && s == 6) send(2);
        if constexpr (i == 8 && s == 7) {
          wait_bar<0>();
          if constexpr (!G) if (r == 0 && lane == 0) __hip_atomic_store(flag + 32 * (33 + h), tag, __ATOMIC_RELAXED, __HIP_MEMORY_SCOPE_AGENT);
        }
        if constexpr (i == 13 && s < 7) {
          if constexpr (!G && OP && s == 0) prev = realtime();
          if constexpr (!G && OP && s > 0 && s < 6) space<OP>(prev);
          if constexpr (s < 6) wait_bar<(s < 5 ? 8 : 4)>();
          if constexpr (s < 4 && !G) issueD(s + 4);
          rdO(f + 3); rdO(f + 4); rdO(f + 5);
          if constexpr (s == 2 || s == 4) take(z - 1);
        }
        if constexpr (i == 14 && s < 7) { rdO(f + 5); rdO(f + 6); }
        if constexpr (i == 15 && s < 7) { rdO(f + 6); if constexpr (s == 2 || s == 4) send(z - 1); }
        SBAR();
        constexpr int p = 4 * (s & 1) + (i >> 2), k = i & 3;
        if constexpr (i == 0 && !(s & 1)) acc[z & 1] = MFMA(cat(wfa[p][k], wfb[p][k]), of[f & 7], (f32x16){});
        else acc[z & 1] = MFMA(cat(wfa[p][k], wfb[p][k]), of[f & 7], acc[z & 1]);
        PIN(acc[z & 1]);
        SBAR();
      });
      if constexpr (s & 1) IT(55 + z);
    });
    give(3);
    if constexpr (!G) if (r == 0) poll(flag + 32 * (33 + 8 * ((ks + 1 + lane % 3) & 3) + nt), tag);
    lbar();
    IT(59);
    if constexpr (!G) if (r == 0 && h == 0 && lane == 0) __hip_atomic_store(flag, tag, __ATOMIC_RELAXED, __HIP_MEMORY_SCOPE_AGENT);
    u32x4 t[6];
    const unsigned so = WS_PART + (nt * 4 + ks) * 3 * HB + wid * 2048;
#pragma unroll
    for (int z = 0; z < 3; ++z) {
      t[2 * z] = __builtin_amdgcn_raw_buffer_load_b128(rsB, lane * 16, so + z * HB, 16);
      t[2 * z + 1] = __builtin_amdgcn_raw_buffer_load_b128(rsB, lane * 16 + 1024, so + z * HB, 16);
    }
    take(3);
    k0 += t0; k1 += t1;
#pragma unroll
    for (int z = 0; z < 3; ++z) { k0 += __builtin_bit_cast(f32x4, t[2 * z]); k1 += __builtin_bit_cast(f32x4, t[2 * z + 1]); }
    const unsigned x0 = cvtpk(k0[0], k0[1]), x1 = cvtpk(k0[2], k0[3]), y0 = cvtpk(k1[0], k1[1]), y1 = cvtpk(k1[2], k1[3]);
    auto r0 = __builtin_amdgcn_permlane32_swap(x0, y0, false, false);
    auto r1 = __builtin_amdgcn_permlane32_swap(x1, y1, false, false);
    const u32x4 w4 = {r0[0], r1[0], r0[1], r1[1]};
    __builtin_amdgcn_raw_buffer_store_b128(w4, rsO, l32 * 2048 + hi * 16 + (nt * 128 + r * 32 + g * 16) * 2, 0, 0);
  }
}

__global__ __launch_bounds__(512, 2) void mha_kernel(const uint16_t* __restrict__ x, const uint16_t* __restrict__ kc,
                                                     const uint16_t* __restrict__ vc, const uint16_t* __restrict__ wq,
                                                     const uint16_t* __restrict__ wo, uint16_t* __restrict__ out, char* __restrict__ ws) {
  extern __shared__ __attribute__((aligned(16))) char smem[];
  const unsigned wid = rfl(threadIdx.x >> 6);
  const unsigned lds0 = rfl((unsigned)(uintptr_t)smem);
  if (wid >> 2) body<1>(x, kc, vc, wq, wo, out, ws, lds0, wid & 3);
  else body<0>(x, kc, vc, wq, wo, out, ws, lds0, wid & 3);
}

extern "C" void kernel_launch(void* const* d_in, const int* in_sizes, int n_in, void* d_out, int out_size, void* d_ws,
                              size_t ws_size, hipStream_t stream) {
  (void)hipFuncSetAttribute((const void*)mha_kernel, hipFuncAttributeMaxDynamicSharedMemorySize, LDSB);
  hipLaunchKernelGGL(mha_kernel, dim3(256), dim3(512), LDSB, stream, (const uint16_t*)d_in[0], (const uint16_t*)d_in[1],
                     (const uint16_t*)d_in[2], (const uint16_t*)d_in[3], (const uint16_t*)d_in[4], (uint16_t*)d_out, (char*)d_ws);
}
```

```cpp
#include <hip/hip_runtime.h>
#include <hip/hip_bf16.h>
#include <stdint.h>
#include <utility>
#include <type_traits>

typedef float f32x16 __attribute__((ext_vector_type(16)));
typedef float f32x8 __attribute__((ext_vector_type(8)));
typedef float f32x4 __attribute__((ext_vector_type(4)));
typedef float f32x2 __attribute__((ext_vector_type(2)));
typedef __bf16 bf16x8 __attribute__((ext_vector_type(8)));
typedef __bf16 bf16x2 __attribute__((ext_vector_type(2)));
typedef short s16x4 __attribute__((ext_vector_type(4)));
typedef unsigned u32x4 __attribute__((ext_vector_type(4)));
typedef unsigned u32x2 __attribute__((ext_vector_type(2)));
typedef unsigned long long u64;
typedef __amdgpu_buffer_rsrc_t rsrc_t;
typedef __attribute__((address_space(1))) u64 gu64;

#define LAS __attribute__((address_space(3)))
#define DI __device__ __forceinline__
#define MFMA(a, b, c) __builtin_amdgcn_mfma_f32_16x16x32_bf16((a), (b), (c), 0, 0, 0)
#define PAGE() asm volatile("mha_page")
#define SBAR() do { __builtin_amdgcn_sched_barrier(0); PAGE(); __builtin_amdgcn_sched_barrier(0); } while (0)
#define PIN(x) asm volatile("mha_page" : "+v"(x))
#define BF(x) __builtin_bit_cast(bf16x8, x)
#define IC(n) std::integral_constant<int, (n)>{}

constexpr int SQ = 128, DM = 1024, NH = 32, SK = 2048, DH = 128;
constexpr unsigned HB = 16384;
constexpr int LDSB = 10 * HB;
constexpr float CEXP = 0.12751743082459868f;
constexpr float SUMLIM = 1024.0f;
constexpr unsigned WS_BATCH = 4u << 20, WS_FLAG = 1u << 20, WS_PART = WS_FLAG + 65536, WS_IDLE = 3u << 20;
constexpr unsigned SPIN_CAP = 1u << 18;

constexpr int HK0 = 11, HK1 = 12, HK2 = 14, HV0 = 13, HV1 = 15, QSK = 3;
constexpr unsigned RAMP = 0x1234000;
constexpr int VISIT = 2, VMIN = 30, TAGI = 3;
constexpr int PACE = 110, PLAST = 94, PMIN = 100, PMINL = 85, PDEL = 330, PBASE = 12, LEAK = 300;

DI unsigned rfl(unsigned x) { return __builtin_amdgcn_readfirstlane(x); }
DI rsrc_t mkrsrc(const void* p, unsigned bytes) { return __builtin_amdgcn_make_buffer_rsrc((void*)p, (short)0, (int)bytes, 0x00020000); }
template <int N> DI void wait_barh() {
  asm volatile("mha_page" ::: "memory");
  __builtin_amdgcn_s_waitcnt((N & 15) | ((N >> 4) << 14) | 0x70);
  asm volatile("s_barrier" ::: "memory");
}
template <int N> DI void wait_bar() { asm volatile("mha_page\n\ts_waitcnt vmcnt(%0) lgkmcnt(0)\n\ts_barrier" ::"i"(N) : "memory"); }
DI void lbar() { asm volatile("mha_page\n\ts_waitcnt lgkmcnt(0)\n\ts_barrier" ::: "memory"); }
DI bf16x8 lds_b128(unsigned addr) { return *(const LAS bf16x8*)(uintptr_t)addr; }
DI f32x4 lds_f4(unsigned addr) { return *(const LAS f32x4*)(uintptr_t)addr; }
DI void sts_f4(unsigned addr, f32x4 v) { *(LAS f32x4*)(uintptr_t)addr = v; }
DI s16x4 lds_tr(unsigned addr) { return __builtin_amdgcn_ds_read_tr16_b64_v4i16((LAS s16x4*)(uintptr_t)addr); }
DI bf16x8 cat(s16x4 a, s16x4 b) { return __builtin_bit_cast(bf16x8, __builtin_shufflevector(a, b, 0, 1, 2, 3, 4, 5, 6, 7)); }
DI unsigned cvtpk(float lo, float hi) {
  f32x2 v = {lo, hi};
  return __builtin_bit_cast(unsigned, __builtin_convertvector(v, bf16x2));
}
DI u32x4 pack8(const f32x4& a, const f32x4& b) { return (u32x4){cvtpk(a[0], a[1]), cvtpk(a[2], a[3]), cvtpk(b[0], b[1]), cvtpk(b[2], b[3])}; }
DI u32x4 flip(bool c, u32x4 v) { return (u32x4){c ? v[2] : v[0], c ? v[3] : v[1], c ? v[0] : v[2], c ? v[1] : v[3]}; }
DI f32x4 quad(const f32x16& v, int s) { return (f32x4){v[4 * s], v[4 * s + 1], v[4 * s + 2], v[4 * s + 3]}; }
DI f32x4 quad(const f32x8& v, int s) { return (f32x4){v[4 * s], v[4 * s + 1], v[4 * s + 2], v[4 * s + 3]}; }
DI void mq(f32x16& acc, int s, bf16x8 a, bf16x8 b, f32x4 c) {
  const f32x4 d = MFMA(a, b, c);
  acc[4 * s] = d[0]; acc[4 * s + 1] = d[1]; acc[4 * s + 2] = d[2]; acc[4 * s + 3] = d[3];
}
DI void mq(f32x16& acc, int s, bf16x8 a, bf16x8 b) { mq(acc, s, a, b, quad(acc, s)); }
DI f32x16 alt(float a, float b) { return (f32x16){a, a, a, a, b, b, b, b, a, a, a, a, b, b, b, b}; }
DI float all_max(float v) {
  auto a = __builtin_amdgcn_permlane32_swap(__float_as_uint(v), __float_as_uint(v), false, false);
  v = fmaxf(__uint_as_float(a[0]), __uint_as_float(a[1]));
  auto c = __builtin_amdgcn_permlane16_swap(__float_as_uint(v), __float_as_uint(v), false, false);
  return fmaxf(__uint_as_float(c[0]), __uint_as_float(c[1]));
}
DI float all_sum(float v) {
  auto a = __builtin_amdgcn_permlane32_swap(__float_as_uint(v), __float_as_uint(v), false, false);
  v = __uint_as_float(a[0]) + __uint_as_float(a[1]);
  auto c = __builtin_amdgcn_permlane16_swap(__float_as_uint(v), __float_as_uint(v), false, false);
  return __uint_as_float(c[0]) + __uint_as_float(c[1]);
}
#define DMA4(pol) asm volatile("mha_page\n\ts_mov_b32 m0, %0\n\ts_nop 0\n\t" \
    "buffer_load_dwordx4 %1, %5, %6 offen" pol " lds\n\t" \
    "buffer_load_dwordx4 %2, %5, %7 offen offset:1024" pol " lds\n\t" \
    "buffer_load_dwordx4 %3, %5, %8 offen offset:2048" pol " lds\n\t" \
    "buffer_load_dwordx4 %4, %5, %9 offen offset:3072" pol " lds\n\tmha_page" \
    ::"s"(dst), "v"(va), "v"(vb), "v"(vc_), "v"(vd), "s"(rs), "s"(s0), "s"(s1), "s"(s2), "s"(s3) : "memory")
template <int POL>
DI void dma4(unsigned dst, unsigned va, unsigned vb, unsigned vc_, unsigned vd, rsrc_t rs, unsigned s0, unsigned s1, unsigned s2, unsigned s3) {
  if constexpr (POL == 1) DMA4(" nt");
  else if constexpr (POL == 2) DMA4(" sc1");
  else DMA4("");
}
template <int OFF> DI void dma1(unsigned dst, unsigned v, rsrc_t rs, unsigned so) {
  asm volatile("s_mov_b32 m0, %0\n\ts_nop 0\n\tbuffer_load_dwordx4 %1, %2, %3 offen offset:%4 sc1 lds" ::"s"(dst), "v"(v), "s"(rs), "s"(so), "i"(OFF) : "memory");
}
template <class F, int... Is> DI void sfor_impl(F&& f, std::integer_sequence<int, Is...>) { (f(std::integral_constant<int, Is>{}), ...); }
template <int N, class F> DI void sfor(F&& f) { sfor_impl(f, std::make_integer_sequence<int, N>{}); }
DI unsigned realtime() { u64 t; asm volatile("s_memrealtime %0\n\ts_waitcnt lgkmcnt(0)" : "=s"(t)::"memory"); return (unsigned)t; }
template <int PM> DI void pace(unsigned& due, unsigned& last, unsigned pp) {
  asm volatile("s_add_u32 %0, %0, %2\n\ts_add_u32 %1, %1, %4\n\ts_sub_u32 vcc_hi, %1, %0\n\ts_cmp_gt_i32 vcc_hi, 0\n\ts_cselect_b32 %1, %1, %0\n\tmha_page\n"
               "1:\n\ts_memrealtime vcc\n\ts_waitcnt lgkmcnt(0)\n\ts_sub_u32 vcc_hi, vcc_lo, %1\n\ts_cmp_lt_i32 vcc_hi, 0\n\ts_cbranch_scc1 1b\n\t"
               "mha_page\n\ts_mov_b32 %1, vcc_lo\n\ts_sub_u32 vcc_hi, vcc_lo, %0\n\ts_cmp_gt_i32 vcc_hi, %3\n\ts_cselect_b32 %0, vcc_lo, %0"
               : "+s"(due), "+s"(last) : "s"(pp), "i"(LEAK), "i"(PM) : "memory", "scc", "vcc");
}
#define TOUCH(first, k) do { u64 t_; asm volatile("s_mov_b64 exec, 0\n\ts_getpc_b64 vcc\n1:\n\t" \
    "s_lshl2_add_u32 vcc_lo, %1, vcc_lo\n\ts_addc_u32 vcc_hi, vcc_hi, 0\n\t" \
    "s_sub_u32 vcc_lo, vcc_lo, 1b - .Lmha_tab - 4 * (" first ")\n\ts_subb_u32 vcc_hi, vcc_hi, 0\n\t" \
    "s_mov_b64 %0, vcc\n\ts_swappc_b64 vcc, %0\n\ts_mov_b64 exec, -1" : "=s"(t_) : "s"(k) : "vcc", "scc"); } while (0)
DI void poll(const gu64* p, u64 tag) {
#pragma unroll 1
  for (unsigned n = 0; n < SPIN_CAP; ++n) {
    const u64 v = __hip_atomic_load(p, __ATOMIC_RELAXED, __HIP_MEMORY_SCOPE_AGENT);
    if (__all(v == tag)) break;
    __builtin_amdgcn_s_sleep(2);
  }
}

constexpr int hsite(int G, int k) { return G ? (k == 0 ? HV0 : k == 1 ? HV1 : 99) : (k == 0 ? HK0 : k == 1 ? HK1 : k == 2 ? HK2 : k == 3 ? 16 : 99); }
constexpr int hyoung(int G, int id, int upto) {
  int seq[64] = {}, cnt[64] = {}, n = 0;
  seq[n] = 0; cnt[n++] = 4; seq[n] = 1; cnt[n++] = 4;
  seq[n] = 90; cnt[n++] = G ? 2 : 3;
  for (int it = -1; it < upto; ++it) {
    if (it + 3 <= 15 && (G || it <= QSK)) { seq[n] = it + 3; cnt[n++] = 4; }
    if (it + 4 <= 15 && !G && it >= QSK) { seq[n] = it + 4; cnt[n++] = 4; }
    for (int k = 0; k < 4; ++k) if (hsite(G, k) == it) { seq[n] = 16 + k; cnt[n++] = 4; }
  }
  int y = 0;
  for (int i = n - 1; i >= 0; --i) { if (seq[i] == id) return y; y += cnt[i]; }
  return -1;
}
static_assert(HK0 >= 11 && HK0 < 14 && QSK <= 11 && TAGI >= 1 && TAGI <= 6 && HK1 >= 12 && HK0 <= HK1 && HK1 <= HK2 && HK2 >= 13 && HK2 <= 14 && HV0 <= HV1 && HV1 <= 15, "head schedule");
constexpr unsigned qblock(int G, int i) {
  if (G) return (i % 3 == 0 ? 9 : i % 3 == 1 ? 5 : 7) * HB;
  return (i == 11 ? 0 : i == 12 ? 1 : i == 15 || i % 4 == 0 ? 8 : i % 4 == 1 ? 2 : i % 4 == 2 ? 6 : 1) * HB;
}

struct QFrag { bf16x8 xb[8]; s16x4 w[8]; };

template <int G>
DI void body(const uint16_t* __restrict__ x, const uint16_t* __restrict__ kc, const uint16_t* __restrict__ vc, const uint16_t* __restrict__ wq,
             const uint16_t* __restrict__ wo, uint16_t* __restrict__ out, char* __restrict__ ws, unsigned lds0, unsigned r, rsrc_t rsQ, unsigned voffQ, unsigned voffQ1) {
  constexpr unsigned g = G;
  const unsigned lane = threadIdx.x & 63, wid = 4 * g + r;
  const unsigned bid = blockIdx.x, xcd = bid & 7, idx = bid >> 3;
  const unsigned h = 4 * xcd + (idx & 3), b = idx >> 2;

  const unsigned l4 = lane >> 4;
  const size_t kvoff = (size_t)(b * NH + h) * SK * DH;
  const rsrc_t rsKV = mkrsrc((g ? vc : kc) + kvoff, SK * DH * 2);
  const unsigned voffA = (16 * r + l4) * 256 + 16 * ((lane & 15) ^ (g ? (l4 << 2) : (l4 | ((r & 1) << 3))));
  const unsigned voffB = voffA ^ (g ? 32 : 64);
  constexpr unsigned qstS = g ? 64 * 8192 : 128, qstJ = (g ? 4 * 8192 : 8 * 2048) - 1024;
  const unsigned dstw = lds0 + r * 4096;
  const unsigned kstart = (2 * xcd + 4 * b) & 15;
  auto issueQ = [&](unsigned i, unsigned blk_) {
    const unsigned so = ((i + kstart) & 15) * qstS;
    dma4<0>(dstw + blk_, voffQ, g ? voffQ : voffQ1, g ? voffQ1 : voffQ, voffQ1, rsQ, so, so + qstJ, so + 2 * qstJ, so + 3 * qstJ);
  };
  auto issueKV = [&](unsigned so, int t3) {
    dma4<1>(dstw + (3 * g + t3) * HB, voffA, voffA, voffB, voffB, rsKV, so, so, so, so);
  };


  const unsigned ks = h >> 3, nt = h & 7, oq = ks;
  char* const wsb = ws + (size_t)b * WS_BATCH;
  const rsrc_t rsB = mkrsrc(wsb, WS_BATCH);
  const rsrc_t rsO = mkrsrc(out + (size_t)(b * SQ + 32 * oq) * DM, 32 * DM * 2);

  const unsigned c16 = lane & 15, q4 = c16 >> 2, p4 = lane & 3, uo = l4 & 1, dg = 2 * g + (r & 1);
  const unsigned baseX = lds0 + 128 * (32 * r + c16) + 16 * (l4 ^ (c16 >> 1));
  const unsigned baseW = lds0 + 256 * (8 * l4 + q4) + 16 * (4 * (dg ^ q4) + (p4 ^ uo)) + 8 * (p4 & 1);
  const unsigned baseK = lds0 + 8192 * g + 256 * (8 * q4 + p4) + 16 * (l4 ^ c16 ^ (4 * dg));
  const unsigned baseV = lds0 + 3 * HB + 8192 * g + 256 * (8 * l4 + q4) + 16 * (4 * q4 + 2 * uo + (p4 >> 1)) + 8 * (p4 & 1);
  const unsigned baseT = lds0 + 256 * (4 * l4 + q4) + 16 * (4 * (r ^ q4) + (p4 ^ uo)) + 8 * (p4 & 1);

  f32x4 qa[8] = {};
  QFrag fr[2];
  unsigned aX[4][2], aW[2][2];
#pragma unroll
  for (int k = 0; k < 4; ++k) { aX[k][0] = baseX ^ ((k >> 1) * 64 + (k & 1) * 4096); aX[k][1] = aX[k][0] + 6 * HB; PIN(aX[k][0]); PIN(aX[k][1]); }
#pragma unroll
  for (int k = 0; k < 2; ++k) { aW[k][0] = (baseW ^ (k * 8)) + 5 * HB; aW[k][1] = aW[k][0] + 4 * HB; PIN(aW[k][0]); PIN(aW[k][1]); }
  auto qiter = [&](QFrag& cur, QFrag& nxt, unsigned bx, unsigned bw, bool mm) {
    const bool hx = bx >= 6 * HB, hw = bw >= 9 * HB;
#pragma unroll
    for (int s = 0; s < 8; ++s) {
      nxt.xb[s] = lds_b128(aX[s >> 1][hx] + bx - (hx ? 6 * HB : 0) + (s & 1) * 2048);
      SBAR();
      if (mm) { qa[2 * (s & 3)] = MFMA(cat(cur.w[s & 4], cur.w[(s & 4) + 1]), cur.xb[s], qa[2 * (s & 3)]); PIN(qa[2 * (s & 3)]); }
      SBAR();
      nxt.w[s] = lds_tr(aW[(s >> 1) & 1][hw] + bw - (hw ? 9 * HB : 5 * HB) + 8192 * (s >> 2) + (s & 1) * 1024);
      SBAR();
      if (mm) { qa[2 * (s & 3) + 1] = MFMA(cat(cur.w[(s & 4) + 2], cur.w[(s & 4) + 3]), cur.xb[s], qa[2 * (s & 3) + 1]); PIN(qa[2 * (s & 3) + 1]); }
      SBAR();
    }
  };
  auto hslot = [&](auto ic) {
    constexpr int it = decltype(ic)::value;
    if constexpr (it + 3 <= 15 && (G || it <= QSK)) issueQ(it + 3, qblock(G, it + 3));
    if constexpr (it + 4 <= 15 && !G && it >= QSK) issueQ(it + 4, qblock(G, it + 4));
    sfor<4>([&](auto kc_) {
      constexpr int k = decltype(kc_)::value;
      if constexpr (hsite(G, k) == it) issueKV(k * HB, k % 3);
    });
  };
  f32x16 o[4];
  unsigned aK[4], aV[8];
  auto visit = [&]() {
    if constexpr (G) TOUCH("(.Lmha_l1 - mha_kernel) >> 12", r);
    else {
      u64 t0;
      asm volatile("s_memrealtime %0" : "=s"(t0));
      TOUCH("((.Lmha_e0 - mha_kernel) >> 12) - 3", r);
      asm volatile("s_waitcnt lgkmcnt(0)" : "+s"(t0));
      while (realtime() - (unsigned)t0 < VMIN) {}
    }
    SBAR();
  };
  u64 tag = 0;
  unsigned due = 0, prev = 0, pp = PACE;
  SBAR();
  if constexpr (!G) {
    const unsigned ve = lane < 2 ? WS_FLAG + 4 * lane : WS_IDLE;
    asm volatile("s_mov_b32 m0, %0\n\ts_nop 0\n\tbuffer_load_dword %1, %2, 0 offen sc1 lds" ::"s"(dstw), "v"(ve), "s"(rsB) : "memory");
    asm volatile("s_mov_b32 m0, %0\n\ts_nop 0\n\tbuffer_load_dword off, %1, 0 lds" ::"s"(dstw + 256), "s"(rsO) : "memory");
  } else {
    const rsrc_t rsT = mkrsrc(wo + (size_t)ks * 1024 * DM, 1024 * DM * 2);
    asm volatile("s_mov_b32 m0, %0\n\ts_nop 0\n\tbuffer_load_dword %1, %2, 0 offen lds" ::"s"(dstw + 3 * HB), "v"((lane & 32) * 32768), "s"(rsT) : "memory");
  }
  asm volatile("s_mov_b32 m0, %0\n\ts_nop 0\n\tbuffer_load_dword off, %1, 0 nt lds" ::"s"(dstw + 3 * g * HB + 512), "s"(rsKV) : "memory");
  wait_bar<hyoung(G, 0, -1)>();
  hslot(IC(-1));
  qiter(fr[1], fr[0], qblock(0, 0), qblock(1, 0), false);
#pragma unroll
  for (int k = 0; k < 4; ++k) { aK[k] = baseK ^ (k << 6); PIN(aK[k]); }
#pragma unroll
  for (int k = 0; k < 8; ++k) { aV[k] = baseV ^ (k << 5); PIN(aV[k]); if (k & 1) SBAR(); }
#pragma unroll
  for (int i = 0; i < 4; ++i) { o[i] = (f32x16){}; PIN(o[i]); SBAR(); }
  sfor<15>([&](auto ic) {
    constexpr int i = decltype(ic)::value;
    wait_barh<hyoung(G, !G && i == 14 ? 16 : i + 1, i)>();
    if constexpr (G) hslot(IC(i));
    if constexpr (i == VISIT) visit();
    if constexpr (i < 8) if constexpr ((RAMP >> (4 * i)) & 15) { __builtin_amdgcn_s_sleep((RAMP >> (4 * i)) & 15); SBAR(); }
    if constexpr (!G && i == PBASE) { due = realtime() + PDEL; prev = due - 1000; }
    if constexpr (i == TAGI) {
      const u32x2 e = *(const LAS u32x2*)(uintptr_t)dstw;
      tag = (((u64)rfl(e[1]) << 32) | rfl(e[0])) + 0x9E3779B97F4A7C15ull;
      asm volatile("" : "+s"(tag));
    }
    qiter(fr[i & 1], fr[(i + 1) & 1], qblock(0, i + 1), qblock(1, i + 1), true);
    if constexpr (!G) hslot(IC(i));
  });
  bf16x8 qf[8], kf[8];
  {
    QFrag& cur = fr[1];
    const unsigned xw = lds0 + 6 * HB + r * 8192 + dg * 2048 + lane * 16;
    auto mm = [&](int s) {
      kf[s] = lds_b128(aK[s >> 1] + (s & 1) * 1024);
      SBAR();
      qa[2 * (s & 3)] = MFMA(cat(cur.w[s & 4], cur.w[(s & 4) + 1]), cur.xb[s], qa[2 * (s & 3)]); PIN(qa[2 * (s & 3)]);
      qa[2 * (s & 3) + 1] = MFMA(cat(cur.w[(s & 4) + 2], cur.w[(s & 4) + 3]), cur.xb[s], qa[2 * (s & 3) + 1]); PIN(qa[2 * (s & 3) + 1]);
      SBAR();
    };
    auto fin = [&](int k) {
      f32x4 y = qa[2 * k] * CEXP, w = qa[2 * k + 1] * CEXP;
      PIN(y); PIN(w);
      u32x4 v = pack8(y, w);
      PIN(v);
      qf[k] = BF(flip(uo, v)); PIN(qf[k]);
      *(LAS bf16x8*)(uintptr_t)((xw ^ ((k & 2) * 4096)) + (k & 1) * 1024) = qf[k];
      SBAR();
    };
    mm(0); mm(1); mm(4); mm(5); mm(2); mm(6);
    fin(0);
    mm(3);
    fin(1);
    mm(7);
    fin(2);
    fin(3);
    if constexpr (G) lbar(); else wait_bar<hyoung(0, 17, 15)>();
    hslot(IC(15));
#pragma unroll
    for (int k = 2; k < 8; ++k) qf[k] = lds_b128((xw ^ ((k & 6) * 1024)) + (k & 1) * 1024);
    SBAR();
  }

  f32x16 sc;
  f32x8 negm;
  s16x4 va[8], vb[8];
  u32x4 pa0, pa1, pb0, pb1;
  float m0, m1, l0, l1;

  auto rowmax = [&](int q) {
    const f32x4 s = quad(sc, q), t = quad(sc, 2 + q);
    const float a = fmaxf(fmaxf(s[0], s[1]), s[2]), c = fmaxf(fmaxf(s[3], t[0]), t[1]);
    return all_max(fmaxf(fmaxf(fmaxf(a, t[2]), t[3]), c));
  };
  auto probs = [&](int q, u32x4& p) {
    float sacc = 0.f;
#pragma unroll
    for (int i = 0; i < 4; ++i) {
      const int e = 8 * (i >> 1) + 4 * q + 2 * (i & 1);
      const float e0 = __builtin_amdgcn_exp2f(sc[e]), e1 = __builtin_amdgcn_exp2f(sc[e + 1]);
      sacc += e0; sacc += e1;
      PIN(sacc);
      p[i] = cvtpk(e0, e1);
    }
    return sacc;
  };
  auto guard = [&](f32x2 sa, u32x4& p0, u32x4& p1) {
    float s0 = sa[0], s1 = sa[1];
    if (__builtin_expect(__any(!(s0 + s1 <= SUMLIM)), 0)) {
      const float d0 = fmaxf(rowmax(0), 0.f), d1 = fmaxf(rowmax(1), 0.f);
      const float a0 = __builtin_amdgcn_exp2f(-d0), a1 = __builtin_amdgcn_exp2f(-d1);
      m0 += d0; m1 += d1;
      l0 *= a0; l1 *= a1;
      o[0] *= alt(a0, a1); o[1] *= alt(a0, a1); o[2] *= alt(a0, a1); o[3] *= alt(a0, a1);
      sc -= alt(d0, d1); negm -= (f32x8){d0, d0, d0, d0, d1, d1, d1, d1};
      s0 = probs(0, p0); s1 = probs(1, p1);
      PIN(m0); PIN(m1); PIN(l0); PIN(l1); PIN(s0); PIN(s1); PIN(p0); PIN(p1); PIN(negm); PIN(o[0]); PIN(o[1]); PIN(o[2]); PIN(o[3]);
    }
    l0 += s0; PIN(l0); l1 += s1; PIN(l1);
  };
  auto phaseA = [&](unsigned vo) {
    __builtin_amdgcn_s_setprio(1);
    asm volatile("" : "=v"(sc));
#pragma unroll
    for (int k = 0; k < 8; ++k) {
      va[k] = lds_tr(aV[k] + vo); vb[k] = lds_tr(aV[k] + vo + 1024);
      SBAR();
      const int a = 2 * (k & 1);
      mq(sc, a, kf[k], qf[k & 6], k < 2 ? quad(negm, 0) : quad(sc, a));
      mq(sc, a + 1, kf[k], qf[k | 1], k < 2 ? quad(negm, 1) : quad(sc, a + 1));
      PIN(sc);
      SBAR();
    }
  };
  auto phaseB = [&](const u32x4& po0, const u32x4& po1, u32x4& pn0, u32x4& pn1, auto&& rd) {
    float sa[2];
    __builtin_amdgcn_s_setprio(0);
    SBAR();
#pragma unroll
    for (int i = 0; i < 8; ++i) {
      rd(i);
      SBAR();
      const bf16x8 vf = cat(va[i], vb[i]);
      const int q = (i >> 1) & 1, w = 2 * (i >> 2) + (i & 1);
      mq(o[i >> 1], 2 * (i & 1), vf, BF(po0));
      PIN(o[i >> 1]);
      SBAR();
      float e0 = __builtin_amdgcn_exp2f(sc[2 * i]);
      PIN(e0);
      SBAR();
      mq(o[i >> 1], 2 * (i & 1) + 1, vf, BF(po1)); PIN(o[i >> 1]);
      SBAR();
      const float e1 = __builtin_amdgcn_exp2f(sc[2 * i + 1]);
      if (w == 0) sa[q] = e0 + e1; else { sa[q] += e0; sa[q] += e1; }
      if (q == 0) { pn0[w] = cvtpk(e0, e1); PIN(pn0); } else { pn1[w] = cvtpk(e0, e1); PIN(pn1); }
      PIN(sa[q]);
      SBAR();
    }
    asm volatile("" ::: "memory");
    return (f32x2){sa[0], sa[1]};
  };

  {
#pragma unroll
    for (int k = 0; k < 8; ++k) {
      const int a = 2 * (k & 1);
      mq(sc, a, kf[k], qf[k & 6], k < 2 ? (f32x4){} : quad(sc, a));
      mq(sc, a + 1, kf[k], qf[k | 1], k < 2 ? (f32x4){} : quad(sc, a + 1));
    }
    PIN(sc);
    SBAR();
    hslot(IC(16));
#pragma unroll
    for (int k = 0; k < 8; ++k) kf[k] = lds_b128(aK[k >> 1] + HB + (k & 1) * 1024);
    SBAR();
    m0 = rowmax(0); m1 = rowmax(1);
    negm = (f32x8){-m0, -m0, -m0, -m0, -m1, -m1, -m1, -m1};
    sc -= alt(m0, m1);
    PIN(negm); PIN(sc);
    l0 = probs(0, pa0); l1 = probs(1, pa1);
    PIN(pa0); PIN(pa1); PIN(l0); PIN(l1);
    wait_bar<hyoung(G, G ? 16 : 18, 17)>();
  }

  const rsrc_t rsW = mkrsrc(wo + (size_t)(ks * 1024 + g * 64) * DM + nt * 128, (1024 - g * 64) * DM * 2 - nt * 256);
  s16x4 wfa[8][4], wfb[8][4];
  const unsigned b6 = b >= 6 ? b - 6 : b;
  auto chunk = [&](unsigned p) { const unsigned c = p + b6, d = p < 2 ? p : c >= 8 ? c - 6 : c; return p == 0 ? nt : d == nt ? 0 : d; };
  auto issueW = [&](unsigned p, unsigned blk_, unsigned half = 0) {
    const unsigned vw = (16 * r + l4) * 2048 + 16 * ((lane & 15) ^ (l4 << 2));
    const unsigned so = (2 * chunk(p) + half) * (64 * 2048);
    dma4<0>(dstw + blk_ * HB, vw, vw ^ 16, vw, vw ^ 16, rsW, so, so + 7168, so + 2 * 7168, so + 3 * 7168);
  };
  auto readW = [&](int p, int k, unsigned blk_) {
    const unsigned a_ = (baseT ^ ((k & 1) * 8)) + blk_ * HB + 8192 * (k >> 1);
    wfa[p][k] = lds_tr(a_); wfb[p][k] = lds_tr(a_ + 4096);
    if (p > 2) SBAR();
  };

  unsigned nso = (g ? 2 : 4) * HB;
  unsigned wcnt = 0, wso = chunk(0) * (128 * 2048), wdst = dstw + (6 + g) * HB;
  auto step = [&](auto jc, auto lastc) {
    constexpr int j = decltype(jc)::value;
    constexpr bool last = decltype(lastc)::value;
    constexpr bool kv = !last || G || j <= 28;
    constexpr bool wp = !last && j % 3 == (G ? 0 : 2);
    auto issue = [&]() {
      if constexpr (wp) {
        const unsigned vw = (16 * r + l4) * 2048 + 16 * ((lane & 15) ^ (l4 << 2));
        const unsigned pc = wcnt & 3;
        asm volatile("s_mov_b32 m0, %0\n\ts_nop 0\n\tbuffer_load_dwordx4 %1, %2, %3 offen lds" ::"s"(wdst + pc * 1024), "v"(j > 3 ? vw ^ 16 : vw), "s"(rsW), "s"(wso + pc * 8192) : "memory");
        ++wcnt;
      }
      if constexpr (kv) { issueKV(nso, (G ? j + 1 : j) % 3); nso += HB; }
    };
    u32x4& po0 = (j & 1) ? pa0 : pb0; u32x4& po1 = (j & 1) ? pa1 : pb1;
    u32x4& pn0 = (j & 1) ? pb0 : pa0; u32x4& pn1 = (j & 1) ? pb1 : pa1;
    if constexpr (G) issue();
    phaseA(((j + 2) % 3) * HB);
    if constexpr (!G && j == 30) issueW(2, 0, 1);
    const f32x2 sacc = phaseB(po0, po1, pn0, pn1, [&](int i) {
      if (i < 4) { kf[2 * i] = lds_b128(aK[i] + ((j + 1) % 3) * HB); kf[2 * i + 1] = lds_b128(aK[i] + ((j + 1) % 3) * HB + 1024); }
    });
    if constexpr (!G) issue();
    if constexpr (!G && j == 29) issueW(2, 2, 0);
    guard(sacc, pn0, pn1);
    if constexpr (!G) {
      if constexpr (last && j >= 28) pp = PLAST;
      pace<(last ? PMINL : PMIN)>(due, prev, pp);
    }
    constexpr int young = wp ? 5 : !G && j == 30 ? 8 : 4;
    wait_bar<young>();
  };
  for (unsigned jj = 0; jj < 4; ++jj) {
    sfor<6>([&](auto c) { step(IC(decltype(c)::value + 1), std::false_type{}); });
    if (jj == 1) { wso = chunk(1) * (128 * 2048); wdst = dstw + (8 + g) * HB; }
  }
  if constexpr (G) asm volatile(".Lmha_l1:\n\tmha_page"); else PAGE();
  sfor<6>([&](auto c) { step(IC(decltype(c)::value + 25), std::true_type{}); });

  {
    phaseA(0);
    if constexpr (!G) issueW(3, 1);
    const f32x2 sacc = phaseB(pa0, pa1, pb0, pb1, [&](int i) { readW(i >> 2, i & 3, 6 + 2 * (i >> 2) + g); });
    guard(sacc, pb0, pb1);
    const unsigned xl = lds0 + lane * 16, pw = wid ^ 4;
    l0 = all_sum(l0); l1 = all_sum(l1);
    sts_f4(xl + 5 * HB + wid * 1024, (f32x4){m0, m1, l0, l1});
    wait_bar<4 - 4 * G>();
    const f32x4 ml = lds_f4(xl + 5 * HB + pw * 1024);
#pragma unroll
    for (int k = 0; k < 8; ++k) {
      const unsigned a_ = aV[k ^ (4 - 4 * g)] + HB;
      va[k] = lds_tr(a_); vb[k] = lds_tr(a_ + 1024);
      SBAR();
    }
    if constexpr (G) issueW(3, 3);
    float cf[2];
#pragma unroll
    for (int q = 0; q < 2; ++q) {
      const float mq_ = q ? m1 : m0, mf = fmaxf(mq_, ml[q]);
      const float a0 = __builtin_amdgcn_exp2f(mq_ - mf), a1 = __builtin_amdgcn_exp2f(ml[q] - mf);
      cf[q] = a0 * __builtin_amdgcn_rcpf(a0 * (q ? l1 : l0) + a1 * ml[2 + q]);
      PIN(cf[q]);
    }
    f32x4 e[16], u[8];
    SBAR();
#pragma unroll
    for (int k = 0; k < 9; ++k) {
      const int d = k ^ (4 - 4 * g);
      if (k < 8) {
        e[2 * k] = MFMA(cat(va[k], vb[k]), BF(pb0), quad(o[d >> 1], 2 * (d & 1))); PIN(e[2 * k]);
        e[2 * k + 1] = MFMA(cat(va[k], vb[k]), BF(pb1), quad(o[d >> 1], 2 * (d & 1) + 1)); PIN(e[2 * k + 1]);
      }
      SBAR();
      if (k >= 1) { e[2 * k - 2] *= cf[0]; e[2 * k - 1] *= cf[1]; PIN(e[2 * k - 2]); PIN(e[2 * k - 1]); }
      if (k >= 1 && k <= 4) {
        sts_f4(xl + 6 * HB + wid * 8192 + (2 * k - 2) * 1024, e[2 * k - 2]);
        sts_f4(xl + 6 * HB + wid * 8192 + (2 * k - 1) * 1024, e[2 * k - 1]);
      }
      if (k >= 4 && k < 8) readW(2, k - 4, 2 - 2 * g);
      SBAR();
    }
    wait_bar<0>();
#pragma unroll
    for (int i = 0; i < 8; ++i) u[i] = lds_f4(xl + 6 * HB + pw * 8192 + ((i & 4) + (i & 1) * 2 + ((i >> 1) & 1)) * 1024);
    SBAR();
    const unsigned vo = ((h * 4 + r) * 8 + 4 * g) * 1024 + lane * 16;
    u32x4 pk[4];
#pragma unroll
    for (int k = 0; k < 4; ++k) {
      const int a = 8 + 4 * (k >> 1) + (k & 1);
      pk[k] = pack8(e[a] + u[2 * k], e[a + 2] + u[2 * k + 1]);
      __builtin_amdgcn_raw_buffer_store_b128(pk[k], rsB, vo + k * 1024, 0, 16);
    }
    issueW(4, 2 * g);
#pragma unroll
    for (int k = 0; k < 4; ++k) *(LAS u32x4*)(uintptr_t)(xl + (4 + g) * HB + r * 4096 + k * 1024) = pk[k];
#pragma unroll
    for (int k = 0; k < 4; ++k) readW(3, k, 2 * g + 1);
    wait_bar<0>();
  }
  gu64* const flag = (gu64*)(wsb + WS_FLAG);
  if constexpr (!G) if (r == 0 && lane < 16) __hip_atomic_store(flag + 32 * (1 + h) + lane, tag, __ATOMIC_RELAXED, __HIP_MEMORY_SCOPE_AGENT);
  f32x4 acc[4][4];
  {
    bf16x8 ow[16];
#pragma unroll
    for (int n = 0; n < 16; ++n) ow[n] = lds_b128(lds0 + lane * 16 + (4 + g) * HB + ((oq + 1 + (n >> 2)) & 3) * 4096 + (n & 3) * 1024);
    SBAR();
    issueW(5, 2 * g + 1); issueW(6, 6 + g); issueW(7, 8 + g);
    SBAR();
#pragma unroll
    for (int n = 0; n < 16; ++n) {
      const int z = n >> 2, k = n & 2, q = n & 1;
      acc[z][q] = MFMA(cat(wfa[0][k], wfb[0][k]), ow[n], k ? acc[z][q] : (f32x4){}); PIN(acc[z][q]);
      acc[z][2 + q] = MFMA(cat(wfa[0][k + 1], wfb[0][k + 1]), ow[n], k ? acc[z][2 + q] : (f32x4){}); PIN(acc[z][2 + q]);
    }
    SBAR();
  }
#pragma unroll
  for (int k = 0; k < 4; ++k) readW(4, k, 2 * g);
  asm volatile("" ::: "memory");
  if constexpr (!G) if (r == 0) poll(flag + 32 * (1 + 8 * ks + (lane & 7)), tag);
  wait_bar<0>();

  auto issueD = [&](unsigned s) {
    const unsigned c = chunk(4 * (s & 1) + r), q = (oq + 1 + (s >> 1)) & 3;
    const unsigned so = (8 * ks + c) * 32768 + q * 8192 + g * 4096;
    const unsigned vl = !(s & 1) && r == 0 ? WS_BATCH : lane * 16;
    dma4<2>(dstw + ((s & 1) ? 2 * g + ((s >> 1) & 1) : 4 + g + (s & 2)) * HB, vl, vl, vl, vl, rsB, so, so, so, so);
  };
  auto issueP = [&](unsigned s, auto kc_) {
    constexpr int k = decltype(kc_)::value;
    const unsigned c = chunk(4 * (s & 1) + r), q = (oq + 1 + (s >> 1)) & 3;
    const unsigned so = (8 * ks + c) * 32768 + q * 8192 + g * 4096;
    const unsigned vl = !(s & 1) && r == 0 ? WS_BATCH : lane * 16;
    dma1<k * 1024>(dstw + ((s & 1) ? 2 * g + ((s >> 1) & 1) : 4 + g + (s & 2)) * HB, vl, rsB, so);
  };
  issueD(0); issueD(1);
#pragma unroll
  for (int k = 0; k < 12; ++k) readW(5 + (k >> 2), k & 3, k < 4 ? 2 * g + 1 : 4 + 2 * (k >> 2) + g);
  wait_bar<4>();
  if constexpr (!G) { issueD(2); issueD(3); }
  {
    unsigned aD[2] = {lds0 + (4 + g) * HB + lane * 16, lds0 + 2 * g * HB + lane * 16};
    PIN(aD[0]); PIN(aD[1]);
    unsigned aR = lds0 + 8 * HB + r * 4096 + lane * 16;
    PIN(aR);
    u64 fv = 0;
    bf16x8 of[8];
    f32x4 k0, k1, t0, t1;
    auto rdO = [&](int n) {
      const int f = n / 28 * 32 + n % 28 + 4;
      of[n & 7] = lds_b128(aD[(f >> 4) & 1] + ((f >> 5) & 1) * ((f & 16) ? 1 : 2) * HB + (f & 15) * 1024);
    };
    auto give = [&](int z, int c) { sts_f4(aR + (z & 1) * HB + g * 2048 + c * 1024, acc[z][2 * c + 1 - G]); };
    auto take = [&](int z) {
      t0 = lds_f4(aR + (z & 1) * HB + (1 - g) * 2048); t1 = lds_f4(aR + (z & 1) * HB + (1 - g) * 2048 + 1024);
    };
    auto send = [&](int z, int c) {
      const unsigned vo = WS_PART + ((nt * 4 + ((ks + 1 + z) & 3)) * 3 + z) * HB + wid * 2048 + lane * 16;
      f32x4 y = acc[z][2 * c + G];
      const f32x4 w = c ? t1 : t0;
#pragma unroll
      for (int e = 0; e < 4; ++e) { float a = y[e] + w[e]; PIN(a); y[e] = a; }
      __builtin_amdgcn_raw_buffer_store_b128(__builtin_bit_cast(u32x4, y), rsB, vo + c * 1024, 0, 16);
      asm volatile("s_nop 1" ::: "memory");
    };
#pragma unroll
    for (int f = 0; f < 6; ++f) rdO(f);
    sfor<8>([&](auto sc_) {
      constexpr int s = decltype(sc_)::value, z = s >> 1;
      sfor<12 + 4 * (s & 1)>([&](auto ic) {
        constexpr int i = decltype(ic)::value + 4 - 4 * (s & 1), f = 28 * z + 16 * (s & 1) + i - 4, t = f - 28 * z;
        if constexpr (i == 11 && s == 0 && G) { issueD(2); issueD(3); }
        if constexpr (i <= 9) rdO(f + 6);
        if constexpr (z >= 1 && t == (z < 3 ? 1 + 2 * G : 1 + G)) give(z - 1, 0);
        if constexpr (z >= 1 && t == (z < 3 ? 2 + 2 * G : 2 + G)) give(z - 1, 1);
        if constexpr (t == 4 && z == 3) {
          wait_bar<0>(); take(2);
          if constexpr (!G) if (r == 0 && lane < 16) __hip_atomic_store(flag + 32 * (33 + h) + 16 + lane, tag, __ATOMIC_RELAXED, __HIP_MEMORY_SCOPE_AGENT);
        }
        if constexpr (z >= 1 && t == (z < 3 ? 11 + 5 * G : 6 + G)) send(z - 1, 0);
        if constexpr (z >= 1 && t == (z < 3 ? 13 + 5 * G : 7 + G)) send(z - 1, 1);
        if constexpr (i == 8 && s == 7) {
          wait_bar<0>();
          if constexpr (!G) if (r == 0 && lane < 16) __hip_atomic_store(flag + 32 * (33 + h) + lane, tag, __ATOMIC_RELAXED, __HIP_MEMORY_SCOPE_AGENT);
          if constexpr (G) fv = __hip_atomic_load(flag + 32 * (33 + 8 * ((ks + 2 + (lane & 1)) & 3) + nt) + 16, __ATOMIC_RELAXED, __HIP_MEMORY_SCOPE_AGENT);
        }
        if constexpr (i == 13 && s < 7) {
          if constexpr (s < 6) wait_bar<(s < 5 ? 8 : 4)>();
          rdO(f + 3); rdO(f + 4); rdO(f + 5);
          if constexpr (s == 2 || s == 4) take(z - 1);
        }
        if constexpr (i == 14 && s < 7) { rdO(f + 5); rdO(f + 6); }
        if constexpr (i == 15 && s < 7) rdO(f + 6);
        constexpr int sb = i >= 13 ? s : s - 1, w = i >= 13 ? i - 14 : i - 2 + 4 * (s & 1), d = (sb & 1) ? 3 : 4;
        if constexpr (sb >= 0 && sb < 4 && w >= 0 && w % d == 0 && w / d < 4) issueP(sb + 4, IC(w / d));
        SBAR();
        constexpr int p = 4 * (s & 1) + (i >> 2), k = i & 2, q = i & 1;
        acc[z][q] = MFMA(cat(wfa[p][k], wfb[p][k]), of[f & 7], acc[z][q]); PIN(acc[z][q]);
        acc[z][2 + q] = MFMA(cat(wfa[p][k + 1], wfb[p][k + 1]), of[f & 7], acc[z][2 + q]); PIN(acc[z][2 + q]);
        SBAR();
      });
    });
    give(3, 0); give(3, 1);
    u32x4 t[6];
    auto fetch = [&](int z) {
      const unsigned so = WS_PART + ((nt * 4 + ks) * 3 + z) * HB + wid * 2048;
      t[2 * z] = __builtin_amdgcn_raw_buffer_load_b128(rsB, lane * 16, so, 16);
      t[2 * z + 1] = __builtin_amdgcn_raw_buffer_load_b128(rsB, lane * 16 + 1024, so, 16);
    };
    if constexpr (G) if (__builtin_expect(!__all(fv == tag), 1)) poll(flag + 32 * (33 + 8 * ((ks + 2 + (lane & 1)) & 3) + nt) + 16, tag);
    lbar();
    fetch(0); fetch(1);
    take(3);
    SBAR();
    if constexpr (!G) if (r == 0) poll(flag + 32 * (33 + 8 * ((ks + 1) & 3) + nt), tag);
    lbar();
    fetch(2);
    if constexpr (!G) if (r == 0 && h == 0 && lane == 0) __hip_atomic_store(flag, tag, __ATOMIC_RELAXED, __HIP_MEMORY_SCOPE_AGENT);
    k0 = acc[3][G] + t0; k1 = acc[3][2 + G] + t1;
#pragma unroll
    for (int z = 0; z < 3; ++z) { k0 += __builtin_bit_cast(f32x4, t[2 * z]); k1 += __builtin_bit_cast(f32x4, t[2 * z + 1]); }
    __builtin_amdgcn_raw_buffer_store_b128(flip(uo, pack8(k0, k1)), rsO, (16 * g + c16) * 2048 + l4 * 16 + (nt * 128 + r * 32) * 2, 0, 0);
  }
}

extern "C" __global__ __launch_bounds__(512, 2) __attribute__((aligned(4096))) void mha_kernel(const uint16_t* __restrict__ x, const uint16_t* __restrict__ kc,
                                                     const uint16_t* __restrict__ vc, const uint16_t* __restrict__ wq,
                                                     const uint16_t* __restrict__ wo, uint16_t* __restrict__ out, char* __restrict__ ws) {
  extern __shared__ __attribute__((aligned(16))) char smem[];
  asm volatile(".set .Lmha_pg, 0\n.macro mha_page\n.if ((. - mha_kernel) >> 12) > .Lmha_pg\n.set .Lmha_pg, (. - mha_kernel) >> 12\n"
               ".irp n,1,2,3,4,5,6,7,8,9,10,11,12,13,14,15,16,17,18,19,20,21,22,23\n.if .Lmha_pg == \\n\n.Lmha_p\\n:\n.endif\n.endr\n"
               "s_cbranch_execz .Lmha_tab\n.endif\n.endm");
  const unsigned wid = rfl(threadIdx.x >> 6);
  const unsigned lds0 = rfl((unsigned)(uintptr_t)smem);
  const unsigned g = wid >> 2, r = wid & 3, lane = threadIdx.x & 63, l4 = lane >> 4;
  const unsigned xcd = blockIdx.x & 7, idx = blockIdx.x >> 3, h = 4 * xcd + (idx & 3), b = idx >> 2;
  unsigned gm = 0u - g;
  asm volatile("" : "+s"(gm));
  auto pick = [&](unsigned v1, unsigned v0) { return v0 ^ ((v0 ^ v1) & gm); };
  unsigned voffQ = pick((16 * r + l4) * 8192 + 16 * ((lane & 15) ^ (l4 << 2)), (32 * r + (lane >> 3)) * 2048 + 16 * ((lane & 7) ^ l4));
  unsigned voffQ1 = voffQ ^ pick(16, 64), voffQb = voffQ ^ pick(0, 64), voffQc = voffQ ^ pick(16, 0);
  const unsigned qs = pick(64 * 8192, 128), qj = pick(4 * 8192 - 1024, 8 * 2048 - 1024);
  unsigned s0 = ((2 * xcd + 4 * b) & 15) * qs, s1 = s0 + qj, s2 = s1 + qj, s3 = s2 + qj, d0 = lds0 + r * 4096 + (8 + g) * HB;
  unsigned qo = pick(h * DH * 2, b * SQ * DM * 2), qn = pick(DM * NH * DH * 2 - h * DH * 2, SQ * DM * 2);
  asm volatile("" : "+v"(voffQ), "+v"(voffQ1), "+v"(voffQb), "+v"(voffQc), "+s"(s0), "+s"(s1), "+s"(s2), "+s"(s3), "+s"(d0), "+s"(qo), "+s"(qn));
  SBAR();
  const u64 px = (u64)x, pw = (u64)wq;
  const rsrc_t rsQ = mkrsrc((const char*)(((u64)pick(pw >> 32, px >> 32) << 32) | pick((unsigned)pw, (unsigned)px)) + qo, qn);
  dma4<0>(d0, voffQ, voffQb, voffQc, voffQ1, rsQ, s0, s1, s2, s3);
  asm volatile("s_branch 1f\n.Lmha_tab:\n\ts_setpc_b64 vcc\n"
               ".irp n,1,2,3,4,5,6,7,8,9,10,11,12,13,14,15,16,17,18,19,20,21,22,23\n\ts_branch .Lmha_p\\n\n.endr\n1:");
  {
    unsigned pg;
    asm("s_mov_b32 %0, ((.Lmha_g0 - mha_kernel) >> 12) - 4" : "=s"(pg));
    TOUCH("0", wid == 3 ? 0 : wid + 1 + (wid < 3 ? 0 : pg));
  }
  dma4<0>(lds0 + r * 4096 + (2 + 3 * g) * HB, voffQ, voffQb, voffQc, voffQ1, rsQ, s0 + qs, s1 + qs, s2 + qs, s3 + qs);
  if (g) { body<1>(x, kc, vc, wq, wo, out, ws, lds0, r, rsQ, voffQ, voffQ1); __builtin_amdgcn_endpgm(); }
  asm volatile(".Lmha_g0:");
  body<0>(x, kc, vc, wq, wo, out, ws, lds0, r, rsQ, voffQ, voffQ1);
  asm volatile(".Lmha_e0:\n.set .Lmha_pg, 99\n.irp n,1,2,3,4,5,6,7,8,9,10,11,12,13,14,15,16,17,18,19,20,21,22,23\n.ifndef .Lmha_p\\n\n.set .Lmha_p\\n, .Lmha_tab\n.endif\n.endr\n"
               ".if (.Lmha_g0 - mha_kernel < 4 * 4096) || (. - mha_kernel > 19 * 4096)\n.error \"page table\"\n.endif");
}

extern "C" void kernel_launch(void* const* d_in, const int* in_sizes, int n_in, void* d_out, int out_size, void* d_ws,
                              size_t ws_size, hipStream_t stream) {
  (void)hipFuncSetAttribute((const void*)mha_kernel, hipFuncAttributeMaxDynamicSharedMemorySize, LDSB);
  hipLaunchKernelGGL(mha_kernel, dim3(256), dim3(512), LDSB, stream, (const uint16_t*)d_in[0], (const uint16_t*)d_in[1],
                     (const uint16_t*)d_in[2], (const uint16_t*)d_in[3], (const uint16_t*)d_in[4], (uint16_t*)d_out, (char*)d_ws);
}
```
